```python
import torch

def _softmax(x, axis=-1):
    return torch.softmax(x, dim=axis)
def _gelu(x):
    return torch.nn.functional.gelu(x, approximate="tanh")
def _silu(x):
    return torch.nn.functional.silu(x)
def _sigmoid(x):
    return torch.sigmoid(x)
def _layernorm(x, axis=-1, eps=1e-5):
    m = x.mean(dim=axis, keepdim=True)
    v = x.var(dim=axis, keepdim=True, unbiased=False)
    return (x - m) / torch.sqrt(v + eps)
def _scatter_add(tgt, idx, src, axis=0):
    return tgt.clone().index_add_(axis, idx, src)
def _segment_sum(src, seg, n):
    out = torch.zeros((n,) + src.shape[1:], dtype=src.dtype)
    return out.index_add_(0, seg, src)
def _pad(x, pw):
    flat = tuple(v for pair in reversed(pw) for v in pair)
    return torch.nn.functional.pad(x, flat)

kernel_name = 'standalone__sparse_matmul_formats_ell__sparse_mat'

def setup_inputs():

    _w = (lambda a: (lambda t: t.to(torch.bfloat16) if t.is_floating_point() else t)(torch.as_tensor(a)))
    M = N = 65536
    D = 64
    K = max(1, int(0.001 * N))
    col_idx = (lambda lo, hi, *s: torch.randint(lo, hi, s))(0, N, M, K)
    vals = (lambda *s: torch.randn(*s, dtype=torch.bfloat16) * 0.05)(M, K)
    lengths = (lambda lo, hi, *s: torch.randint(lo, hi, s))(1, K + 1, M)
    mask = (torch.arange(K)[None, :] < lengths[:, None]).to(vals.dtype)
    vals = vals * mask
    b = (lambda *s: torch.randn(*s, dtype=torch.bfloat16) * 0.05)(N, D)
    return {
        "vals": _w(vals),
        "col_idx": _w(col_idx),
        "b": _w(b),
    }

def reference(vals, col_idx, b):
    gathered = b[col_idx]
    return torch.einsum("mk,mkd->md", vals, gathered)
```

```cpp
#include <hip/hip_runtime.h>
#include <hip/hip_bf16.h>
#include <stdint.h>

typedef unsigned int u32;
typedef __attribute__((ext_vector_type(4))) unsigned int u32x4;

constexpr int KK = 65;
constexpr int DD = 64;

__device__ __forceinline__ float bflo(u32 x) { return __uint_as_float(x << 16); }
__device__ __forceinline__ float bfhi(u32 x) { return __uint_as_float(x & 0xffff0000u); }

__global__ __launch_bounds__(256) void spmm_v0(const unsigned short* __restrict__ vals, const int* __restrict__ idx,
                                               const u32x4* __restrict__ B, u32x4* __restrict__ out, int M) {
    int tid = blockIdx.x * blockDim.x + threadIdx.x;
    int row = tid >> 3;
    int c = tid & 7;
    if (row >= M) return;
    const unsigned short* vr = vals + (size_t)row * KK;
    const int* ir = idx + (size_t)row * KK;
    float acc[8];
#pragma unroll
    for (int j = 0; j < 8; ++j) acc[j] = 0.f;
#pragma unroll 5
    for (int k = 0; k < KK; ++k) {
        unsigned short v = vr[k];
        if ((v & 0x7fff) != 0) {
            int col = ir[k];
            u32x4 g = B[(size_t)col * 8 + c];
            float f = __uint_as_float((u32)v << 16);
            acc[0] += f * bflo(g[0]); acc[1] += f * bfhi(g[0]);
            acc[2] += f * bflo(g[1]); acc[3] += f * bfhi(g[1]);
            acc[4] += f * bflo(g[2]); acc[5] += f * bfhi(g[2]);
            acc[6] += f * bflo(g[3]); acc[7] += f * bfhi(g[3]);
        }
    }
    u32x4 o;
#pragma unroll
    for (int j = 0; j < 4; ++j) {
        __hip_bfloat162 p = __float22bfloat162_rn(make_float2(acc[2 * j], acc[2 * j + 1]));
        o[j] = *reinterpret_cast<u32*>(&p);
    }
    out[(size_t)row * 8 + c] = o;
}

extern "C" void kernel_launch(void* const* d_in, const int* in_sizes, int n_in,
                              void* d_out, int out_size, void* d_ws, size_t ws_size,
                              hipStream_t stream) {
    const unsigned short* vals = (const unsigned short*)d_in[0];
    const int* idx = (const int*)d_in[1];
    const u32x4* B = (const u32x4*)d_in[2];
    int M = out_size / DD;
    int threads = M * 8;
    spmm_v0<<<(threads + 255) / 256, 256, 0, stream>>>(vals, idx, B, (u32x4*)d_out, M);
}
```

```cpp
#include <hip/hip_runtime.h>
#include <stdint.h>

typedef unsigned int u32;
typedef unsigned short u16;
typedef unsigned long long u64;
typedef u32 u32x4 __attribute__((ext_vector_type(4)));
typedef u32 u32x2 __attribute__((ext_vector_type(2)));
typedef float f32x4 __attribute__((ext_vector_type(4)));
typedef __bf16 bf16x8 __attribute__((ext_vector_type(8)));
typedef __bf16 bf16x2_t __attribute__((ext_vector_type(2)));
typedef float f32x2 __attribute__((ext_vector_type(2)));

#define TLW 7
#define TROWS 16
#define DYN_G (1 << 20)
#define DYN_P (2 << 20)
#define CFG_LOGP 4
#define CFG_WPB 16
#define CFG_PRIO 1
#define CFG_T0 400
#define CFG_TS 1400
#define CFG_L1 (-4000)
#define CFG_L2 0
#define CFG_L3 4000

constexpr int KS = 65;
constexpr int ROWS = 16;
constexpr int SLOTS = ROWS * KS;
constexpr int LOGP = CFG_LOGP;
constexpr int WPB = CFG_WPB;
constexpr int COLS_B = 2432;
constexpr int VR_B = 4608 + 256;
constexpr int S_B = 2048;
constexpr int CNT_B = 256;
constexpr int WAVE_B = COLS_B + VR_B + S_B + CNT_B;
constexpr int OFF_VR = COLS_B, OFF_S = COLS_B + VR_B, OFF_CNT = COLS_B + VR_B + S_B;

__device__ __forceinline__ u32 cvt_pk_bf16(float a, float b) {
    f32x2 v = {a, b};
    bf16x2_t r = __builtin_convertvector(v, bf16x2_t);
    return __builtin_bit_cast(u32, r);
}

template <int CTRL, int RM, int BM>
__device__ __forceinline__ u32 dpp_add(u32 x) {
    return x + (u32)__builtin_amdgcn_update_dpp(0, (int)x, CTRL, RM, BM, true);
}

__device__ __forceinline__ void compute_step(const u32x4 (&g)[4], const u32x4 S, f32x4 (&acc)[4], const u64 lomask) {
    u32x4 F0, F1, F2, F3;
#pragma unroll
    for (int t = 0; t < 4; ++t) {
        u32 X0, X1, Y0, Y1;
        asm("s_mov_b64 vcc, %8\n\t"
            "v_cndmask_b32_dpp %0, %6, %4, vcc row_ror:8 row_mask:0xf bank_mask:0xf\n\t"
            "v_cndmask_b32_dpp %1, %7, %5, vcc row_ror:8 row_mask:0xf bank_mask:0xf\n\t"
            "s_not_b64 vcc, vcc\n\t"
            "v_cndmask_b32_dpp %2, %4, %6, vcc row_ror:8 row_mask:0xf bank_mask:0xf\n\t"
            "v_cndmask_b32_dpp %3, %5, %7, vcc row_ror:8 row_mask:0xf bank_mask:0xf"
            : "=&v"(X0), "=&v"(X1), "=&v"(Y0), "=&v"(Y1)
            : "v"(g[t][0]), "v"(g[t][1]), "v"(g[t][2]), "v"(g[t][3]), "s"(lomask)
            : "vcc");
        F0[t] = __builtin_amdgcn_perm(Y0, X0, 0x05040100u);
        F1[t] = __builtin_amdgcn_perm(Y0, X0, 0x07060302u);
        F2[t] = __builtin_amdgcn_perm(Y1, X1, 0x05040100u);
        F3[t] = __builtin_amdgcn_perm(Y1, X1, 0x07060302u);
    }
    const bf16x8 a = __builtin_bit_cast(bf16x8, S);
    acc[0] = __builtin_amdgcn_mfma_f32_16x16x32_bf16(a, __builtin_bit_cast(bf16x8, F0), acc[0], 0, 0, 0);
    acc[1] = __builtin_amdgcn_mfma_f32_16x16x32_bf16(a, __builtin_bit_cast(bf16x8, F1), acc[1], 0, 0, 0);
    acc[2] = __builtin_amdgcn_mfma_f32_16x16x32_bf16(a, __builtin_bit_cast(bf16x8, F2), acc[2], 0, 0, 0);
    acc[3] = __builtin_amdgcn_mfma_f32_16x16x32_bf16(a, __builtin_bit_cast(bf16x8, F3), acc[3], 0, 0, 0);
}

__device__ __forceinline__ void issue_gather(u32x4 (&g)[4], const char* __restrict__ B, u32x2 c, u32 laneoff) {
    u32 o0, o1, o2, o3;
    asm("v_mad_u32_u16 %0, %1, %2, %3" : "=v"(o0) : "v"(c[0]), "s"(128), "v"(laneoff));
    asm("v_mad_u32_u16 %0, %1, %2, %3 op_sel:[1,0,0,0]" : "=v"(o1) : "v"(c[0]), "s"(128), "v"(laneoff));
    asm("v_mad_u32_u16 %0, %1, %2, %3" : "=v"(o2) : "v"(c[1]), "s"(128), "v"(laneoff));
    asm("v_mad_u32_u16 %0, %1, %2, %3 op_sel:[1,0,0,0]" : "=v"(o3) : "v"(c[1]), "s"(128), "v"(laneoff));
    g[0] = *(const u32x4*)(B + o0);
    g[1] = *(const u32x4*)(B + o1);
    g[2] = *(const u32x4*)(B + o2);
    g[3] = *(const u32x4*)(B + o3);
}

__device__ __forceinline__ u32 ticks_since(u64 t0) { return (u32)(__builtin_amdgcn_s_memrealtime() - t0); }

__global__ __launch_bounds__(WPB * 64) void spmm_ell_kernel(const u16* __restrict__ vals, const u32* __restrict__ idx,
                                                            const char* __restrict__ B, u32x2* __restrict__ out
                                                            ) {
    __shared__ __attribute__((aligned(16))) char smem[WPB * WAVE_B];
    const int lane = threadIdx.x & 63;
    const int wib = __builtin_amdgcn_readfirstlane(threadIdx.x >> 6);
    const u32 tile = blockIdx.x * WPB + wib;
    const u64 t0 = __builtin_amdgcn_s_memrealtime();
    if (CFG_PRIO == 2) {
        const int gen = (WPB == 16) ? (wib >> 2) : ((wib >> 2) + 2 * ((blockIdx.x >> 8) & 1));
        if (gen == 1) __builtin_amdgcn_s_setprio(1);
        else if (gen == 2) __builtin_amdgcn_s_setprio(2);
        else if (gen == 3) __builtin_amdgcn_s_setprio(3);
    }
    const u32* ip = idx + (size_t)tile * SLOTS;
    const u16* vp = vals + (size_t)tile * SLOTS;
    u32x4 I[4]; u32x2 V[4];
#pragma unroll
    for (int i = 0; i < 4; ++i) {
        V[i] = __builtin_nontemporal_load((const u32x2*)(vp + 256 * i + 4 * lane));
        I[i] = __builtin_nontemporal_load((const u32x4*)(ip + 256 * i + 4 * lane));
    }
    const u32 Vt = __builtin_nontemporal_load(vp + 1024 + (lane & 15));
    const u32 It = __builtin_nontemporal_load(ip + 1024 + (lane & 15));

    char* wb = smem + wib * WAVE_B;
    u16* cols = (u16*)wb;
    u32* vr = (u32*)(wb + OFF_VR);
    char* simg = wb + OFF_S;
    u32* cnt = (u32*)(wb + OFF_CNT);
    {
        u32x4 z = {0, 0, 0, 0};
        *(u32x4*)(simg + lane * 16) = z;
        *(u32x4*)(simg + 1024 + lane * 16) = z;
        u32x4 f = {0xffffffffu, 0xffffffffu, 0xffffffffu, 0xffffffffu};
        *(u32x4*)(wb + lane * 16) = f;
        *(u32x4*)(wb + 1024 + lane * 16) = f;
        if (lane < (COLS_B - 2048) / 16) *(u32x4*)(wb + 2048 + lane * 16) = f;
        cnt[lane] = 0;
    }
    u32 rank[17];
#pragma unroll
    for (int i = 0; i < 4; ++i) {
#pragma unroll
        for (int u = 0; u < 4; ++u) {
            const u32 vv = V[i][u >> 1];
            const bool nz = (u & 1) ? ((vv & 0x7fff0000u) != 0) : ((vv & 0x7fffu) != 0);
            rank[4 * i + u] = 0;
            if (nz) rank[4 * i + u] = __hip_atomic_fetch_add(&cnt[(I[i][u] & 0xffffu) >> (16 - LOGP)], 1u, __ATOMIC_RELAXED, __HIP_MEMORY_SCOPE_WORKGROUP);
        }
    }
    const bool nzt = ((Vt & 0x7fffu) != 0) && (lane < 16);
    rank[16] = 0;
    if (nzt) rank[16] = __hip_atomic_fetch_add(&cnt[(It & 0xffffu) >> (16 - LOGP)], 1u, __ATOMIC_RELAXED, __HIP_MEMORY_SCOPE_WORKGROUP);
    const u32 c = cnt[lane];
    u32 x = c;
    x = dpp_add<0x111, 0xf, 0xf>(x);
    x = dpp_add<0x112, 0xf, 0xf>(x);
    x = dpp_add<0x114, 0xf, 0xf>(x);
    x = dpp_add<0x118, 0xf, 0xf>(x);
    x = dpp_add<0x142, 0xa, 0xf>(x);
    x = dpp_add<0x143, 0xc, 0xf>(x);
    const int n = __builtin_amdgcn_readlane((int)x, 63);
    cnt[lane] = x - c;
#pragma unroll
    for (int i = 0; i < 4; i += 2) {
        u32 base[8];
#pragma unroll
        for (int k = 0; k < 8; ++k) base[k] = cnt[(I[i + (k >> 2)][k & 3] & 0xffffu) >> (16 - LOGP)];
#pragma unroll
        for (int k = 0; k < 8; ++k) {
            const int ii = i + (k >> 2), u = k & 3;
            const u32 vv = V[ii][u >> 1];
            const u32 v16 = (u & 1) ? (vv & 0xffff0000u) : (vv << 16);
            const bool nz = (v16 & 0x7fff0000u) != 0;
            const u32 slot = 256 * ii + 4 * lane + u;
            const u32 row = (slot * 4033u) >> 18;
            const u32 p = base[k] + rank[4 * ii + u];
            if (nz) {
                cols[p] = (u16)I[ii][u];
                vr[p] = v16 | (row << 4);
            }
        }
    }
    {
        const u32 p = cnt[(It & 0xffffu) >> (16 - LOGP)] + rank[16];
        if (nzt) {
            cols[p] = (u16)It;
            vr[p] = (Vt << 16) | (15u << 4);
        }
    }

    if (CFG_PRIO == 1) {
        const int gen = (WPB == 16) ? (wib >> 2) : ((wib >> 2) + 2 * ((blockIdx.x >> 8) & 1));
        if (gen == 1) __builtin_amdgcn_s_setprio(1);
        else if (gen == 2) __builtin_amdgcn_s_setprio(2);
        else if (gen == 3) __builtin_amdgcn_s_setprio(3);
    }
    const u32 laneoff = (lane & 7) * 16;
    const u64 lomask = 0x00ff00ff00ff00ffull;
    const u32 sconst = (lane >> 5) * 1024 + ((lane >> 3) & 3) * 256 + (2 * (lane & 3) + ((lane >> 2) & 1)) * 2;
    const char* colrd = (const char*)cols + (lane >> 3) * 8;
    f32x4 acc[4];
#pragma unroll
    for (int j = 0; j < 4; ++j) acc[j] = (f32x4){0.f, 0.f, 0.f, 0.f};
    const int nsteps = (n + 31) >> 5;
#define PACE(col)
    if (nsteps > 0) {
        u32x4 gA[4], gB[4];
        u32x2 c0 = *(const u32x2*)(colrd);
        u32x2 cB = *(const u32x2*)(colrd + 64);
        u32x2 cA = *(const u32x2*)(colrd + 128);
        u32 wv = vr[lane];
        PACE(__builtin_amdgcn_readfirstlane(c0[0] & 0xffffu));
        issue_gather(gA, B, c0, laneoff);
        __builtin_amdgcn_sched_barrier(0);
        u32 sa = sconst + (wv & 0xffffu);
        if (lane < n) *(u16*)(simg + sa) = (u16)(wv >> 16);
        u32x4 S0 = *(const u32x4*)(simg + lane * 16);
        u32x4 S1 = *(const u32x4*)(simg + 1024 + lane * 16);
        if (lane < n) *(u16*)(simg + sa) = 0;
        wv = vr[64 + lane];
        int b = 0;
        for (; 2 * b + 2 < nsteps; ++b) {
            const int s = 2 * b;
            PACE(__builtin_amdgcn_readfirstlane(cB[0] & 0xffffu));
            issue_gather(gB, B, cB, laneoff);
            __builtin_amdgcn_sched_barrier(0);
            compute_step(gA, S0, acc, lomask);
            __builtin_amdgcn_sched_barrier(0);
            issue_gather(gA, B, cA, laneoff);
                __builtin_amdgcn_sched_barrier(0);
            compute_step(gB, S1, acc, lomask);
            __builtin_amdgcn_sched_barrier(0);
            cB = *(const u32x2*)(colrd + (s + 3) * 64);
            cA = *(const u32x2*)(colrd + (s + 4) * 64);
            const int p = (b + 1) * 64 + lane;
            sa = sconst + (wv & 0xffffu);
            if (p < n) *(u16*)(simg + sa) = (u16)(wv >> 16);
            S0 = *(const u32x4*)(simg + lane * 16);
            S1 = *(const u32x4*)(simg + 1024 + lane * 16);
            if (p < n) *(u16*)(simg + sa) = 0;
            wv = vr[(b + 2) * 64 + lane];
            __builtin_amdgcn_sched_barrier(0);
        }
        if (2 * b + 1 < nsteps) {
            issue_gather(gB, B, cB, laneoff);
            __builtin_amdgcn_sched_barrier(0);
            compute_step(gA, S0, acc, lomask);
            compute_step(gB, S1, acc, lomask);
        } else {
            compute_step(gA, S0, acc, lomask);
        }
    }
    {
        const int q = lane >> 4;
        u32x2* op = out + ((size_t)tile * ROWS + 4 * q) * 16 + (lane & 7) * 2 + ((lane >> 3) & 1);
#pragma unroll
        for (int r = 0; r < 4; ++r) {
            u32x2 o;
            o[0] = cvt_pk_bf16(acc[0][r], acc[1][r]);
            o[1] = cvt_pk_bf16(acc[2][r], acc[3][r]);
            op[r * 16] = o;
        }
    }
}

extern "C" void kernel_launch(void* const* d_in, const int* in_sizes, int n_in,
                              void* d_out, int out_size, void* d_ws, size_t ws_size,
                              hipStream_t stream) {
    const u16* vals = (const u16*)d_in[0];
    const u32* idx = (const u32*)d_in[1];
    const char* B = (const char*)d_in[2];
    const int M = out_size / 64;
    const int tiles = M / ROWS;
    spmm_ell_kernel<<<tiles / WPB, WPB * 64, 0, stream>>>(vals, idx, B, (u32x2*)d_out
        );
}
```

```cpp
#include <hip/hip_runtime.h>
#include <stdint.h>

typedef unsigned int u32;
typedef unsigned short u16;
typedef unsigned long long u64;
typedef u32 u32x4 __attribute__((ext_vector_type(4)));
typedef u32 u32x2 __attribute__((ext_vector_type(2)));
typedef float f32x4 __attribute__((ext_vector_type(4)));
typedef __bf16 bf16x8 __attribute__((ext_vector_type(8)));
typedef __bf16 bf16x2_t __attribute__((ext_vector_type(2)));
typedef float f32x2 __attribute__((ext_vector_type(2)));

#define TLW 7
#define TROWS 16
#define DYN_G (1 << 20)
#define DYN_P (2 << 20)
#define CFG_LOGP 4
#define CFG_WPB 16
#define CFG_PRIO 3
#define CFG_T0 400
#define CFG_TS 1400
#define CFG_L1 (-4000)
#define CFG_L2 0
#define CFG_L3 4000

constexpr int KS = 65;
constexpr int ROWS = 16;
constexpr int SLOTS = ROWS * KS;
constexpr int LOGP = CFG_LOGP;
constexpr int WPB = CFG_WPB;
constexpr int COLS_B = 2432;
constexpr int VR_B = 4608 + 256;
constexpr int S_B = 2048;
constexpr int CNT_B = 256;
constexpr int WAVE_B = COLS_B + VR_B + S_B + CNT_B;
constexpr int OFF_VR = COLS_B, OFF_S = COLS_B + VR_B, OFF_CNT = COLS_B + VR_B + S_B;

__device__ __forceinline__ u32 cvt_pk_bf16(float a, float b) {
    f32x2 v = {a, b};
    bf16x2_t r = __builtin_convertvector(v, bf16x2_t);
    return __builtin_bit_cast(u32, r);
}

template <int CTRL, int RM, int BM>
__device__ __forceinline__ u32 dpp_add(u32 x) {
    return x + (u32)__builtin_amdgcn_update_dpp(0, (int)x, CTRL, RM, BM, true);
}

__device__ __forceinline__ void compute_step(const u32x4 (&g)[4], const u32x4 S, f32x4 (&acc)[4], const u64 lomask) {
    u32x4 F0, F1, F2, F3;
#pragma unroll
    for (int t = 0; t < 4; ++t) {
        u32 X0, X1, Y0, Y1;
        asm("s_mov_b64 vcc, %8\n\t"
            "v_cndmask_b32_dpp %0, %6, %4, vcc row_ror:8 row_mask:0xf bank_mask:0xf\n\t"
            "v_cndmask_b32_dpp %1, %7, %5, vcc row_ror:8 row_mask:0xf bank_mask:0xf\n\t"
            "s_not_b64 vcc, vcc\n\t"
            "v_cndmask_b32_dpp %2, %4, %6, vcc row_ror:8 row_mask:0xf bank_mask:0xf\n\t"
            "v_cndmask_b32_dpp %3, %5, %7, vcc row_ror:8 row_mask:0xf bank_mask:0xf"
            : "=&v"(X0), "=&v"(X1), "=&v"(Y0), "=&v"(Y1)
            : "v"(g[t][0]), "v"(g[t][1]), "v"(g[t][2]), "v"(g[t][3]), "s"(lomask)
            : "vcc");
        F0[t] = __builtin_amdgcn_perm(Y0, X0, 0x05040100u);
        F1[t] = __builtin_amdgcn_perm(Y0, X0, 0x07060302u);
        F2[t] = __builtin_amdgcn_perm(Y1, X1, 0x05040100u);
        F3[t] = __builtin_amdgcn_perm(Y1, X1, 0x07060302u);
    }
    const bf16x8 a = __builtin_bit_cast(bf16x8, S);
    acc[0] = __builtin_amdgcn_mfma_f32_16x16x32_bf16(a, __builtin_bit_cast(bf16x8, F0), acc[0], 0, 0, 0);
    acc[1] = __builtin_amdgcn_mfma_f32_16x16x32_bf16(a, __builtin_bit_cast(bf16x8, F1), acc[1], 0, 0, 0);
    acc[2] = __builtin_amdgcn_mfma_f32_16x16x32_bf16(a, __builtin_bit_cast(bf16x8, F2), acc[2], 0, 0, 0);
    acc[3] = __builtin_amdgcn_mfma_f32_16x16x32_bf16(a, __builtin_bit_cast(bf16x8, F3), acc[3], 0, 0, 0);
}

__device__ __forceinline__ void issue_gather(u32x4 (&g)[4], const char* __restrict__ B, u32x2 c, u32 laneoff) {
    u32 o0, o1, o2, o3;
    asm("v_mad_u32_u16 %0, %1, %2, %3" : "=v"(o0) : "v"(c[0]), "s"(128), "v"(laneoff));
    asm("v_mad_u32_u16 %0, %1, %2, %3 op_sel:[1,0,0,0]" : "=v"(o1) : "v"(c[0]), "s"(128), "v"(laneoff));
    asm("v_mad_u32_u16 %0, %1, %2, %3" : "=v"(o2) : "v"(c[1]), "s"(128), "v"(laneoff));
    asm("v_mad_u32_u16 %0, %1, %2, %3 op_sel:[1,0,0,0]" : "=v"(o3) : "v"(c[1]), "s"(128), "v"(laneoff));
    g[0] = *(const u32x4*)(B + o0);
    g[1] = *(const u32x4*)(B + o1);
    g[2] = *(const u32x4*)(B + o2);
    g[3] = *(const u32x4*)(B + o3);
}

__device__ __forceinline__ u32 ticks_since(u64 t0) { return (u32)(__builtin_amdgcn_s_memrealtime() - t0); }

__global__ __launch_bounds__(WPB * 64) void spmm_ell_kernel(const u16* __restrict__ vals, const u32* __restrict__ idx,
                                                            const char* __restrict__ B, u32x2* __restrict__ out
                                                            ) {
    __shared__ __attribute__((aligned(16))) char smem[WPB * WAVE_B];
    const int lane = threadIdx.x & 63;
    const int wib = __builtin_amdgcn_readfirstlane(threadIdx.x >> 6);
    const u32 tile = blockIdx.x * WPB + wib;
    const u64 t0 = __builtin_amdgcn_s_memrealtime();
    if (CFG_PRIO == 2) {
        const int gen = (WPB == 16) ? (wib >> 2) : ((wib >> 2) + 2 * ((blockIdx.x >> 8) & 1));
        if (gen == 1) __builtin_amdgcn_s_setprio(1);
        else if (gen == 2) __builtin_amdgcn_s_setprio(2);
        else if (gen == 3) __builtin_amdgcn_s_setprio(3);
    }
    const u32* ip = idx + (size_t)tile * SLOTS;
    const u16* vp = vals + (size_t)tile * SLOTS;
    u32x4 I[4]; u32x2 V[4];
#pragma unroll
    for (int i = 0; i < 4; ++i) {
        V[i] = __builtin_nontemporal_load((const u32x2*)(vp + 256 * i + 4 * lane));
        I[i] = __builtin_nontemporal_load((const u32x4*)(ip + 256 * i + 4 * lane));
    }
    const u32 Vt = __builtin_nontemporal_load(vp + 1024 + (lane & 15));
    const u32 It = __builtin_nontemporal_load(ip + 1024 + (lane & 15));

    char* wb = smem + wib * WAVE_B;
    u16* cols = (u16*)wb;
    u32* vr = (u32*)(wb + OFF_VR);
    char* simg = wb + OFF_S;
    u32* cnt = (u32*)(wb + OFF_CNT);
    {
        u32x4 z = {0, 0, 0, 0};
        *(u32x4*)(simg + lane * 16) = z;
        *(u32x4*)(simg + 1024 + lane * 16) = z;
        u32x4 f = {0xffffffffu, 0xffffffffu, 0xffffffffu, 0xffffffffu};
        *(u32x4*)(wb + lane * 16) = f;
        *(u32x4*)(wb + 1024 + lane * 16) = f;
        if (lane < (COLS_B - 2048) / 16) *(u32x4*)(wb + 2048 + lane * 16) = f;
        cnt[lane] = 0;
    }
    u32 rank[17];
#pragma unroll
    for (int i = 0; i < 4; ++i) {
#pragma unroll
        for (int u = 0; u < 4; ++u) {
            const u32 vv = V[i][u >> 1];
            const bool nz = (u & 1) ? ((vv & 0x7fff0000u) != 0) : ((vv & 0x7fffu) != 0);
            rank[4 * i + u] = 0;
            if (nz) rank[4 * i + u] = __hip_atomic_fetch_add(&cnt[(I[i][u] & 0xffffu) >> (16 - LOGP)], 1u, __ATOMIC_RELAXED, __HIP_MEMORY_SCOPE_WORKGROUP);
        }
    }
    const bool nzt = ((Vt & 0x7fffu) != 0) && (lane < 16);
    rank[16] = 0;
    if (nzt) rank[16] = __hip_atomic_fetch_add(&cnt[(It & 0xffffu) >> (16 - LOGP)], 1u, __ATOMIC_RELAXED, __HIP_MEMORY_SCOPE_WORKGROUP);
    const u32 c = cnt[lane];
    u32 x = c;
    x = dpp_add<0x111, 0xf, 0xf>(x);
    x = dpp_add<0x112, 0xf, 0xf>(x);
    x = dpp_add<0x114, 0xf, 0xf>(x);
    x = dpp_add<0x118, 0xf, 0xf>(x);
    x = dpp_add<0x142, 0xa, 0xf>(x);
    x = dpp_add<0x143, 0xc, 0xf>(x);
    const int n = __builtin_amdgcn_readlane((int)x, 63);
    cnt[lane] = x - c;
#pragma unroll
    for (int i = 0; i < 4; i += 2) {
        u32 base[8];
#pragma unroll
        for (int k = 0; k < 8; ++k) base[k] = cnt[(I[i + (k >> 2)][k & 3] & 0xffffu) >> (16 - LOGP)];
#pragma unroll
        for (int k = 0; k < 8; ++k) {
            const int ii = i + (k >> 2), u = k & 3;
            const u32 vv = V[ii][u >> 1];
            const u32 v16 = (u & 1) ? (vv & 0xffff0000u) : (vv << 16);
            const bool nz = (v16 & 0x7fff0000u) != 0;
            const u32 slot = 256 * ii + 4 * lane + u;
            const u32 row = (slot * 4033u) >> 18;
            const u32 p = base[k] + rank[4 * ii + u];
            if (nz) {
                cols[p] = (u16)I[ii][u];
                vr[p] = v16 | (row << 4);
            }
        }
    }
    {
        const u32 p = cnt[(It & 0xffffu) >> (16 - LOGP)] + rank[16];
        if (nzt) {
            cols[p] = (u16)It;
            vr[p] = (Vt << 16) | (15u << 4);
        }
    }

    if (CFG_PRIO == 1) {
        const int gen = (WPB == 16) ? (wib >> 2) : ((wib >> 2) + 2 * ((blockIdx.x >> 8) & 1));
        if (gen == 1) __builtin_amdgcn_s_setprio(1);
        else if (gen == 2) __builtin_amdgcn_s_setprio(2);
        else if (gen == 3) __builtin_amdgcn_s_setprio(3);
    }
    const u32 laneoff = (lane & 7) * 16;
    const u64 lomask = 0x00ff00ff00ff00ffull;
    const u32 sconst = (lane >> 5) * 1024 + ((lane >> 3) & 3) * 256 + (2 * (lane & 3) + ((lane >> 2) & 1)) * 2;
    const char* colrd = (const char*)cols + (lane >> 3) * 8;
    f32x4 acc[4];
#pragma unroll
    for (int j = 0; j < 4; ++j) acc[j] = (f32x4){0.f, 0.f, 0.f, 0.f};
    const int nsteps = (n + 31) >> 5;
#define PACE(col) { \
        const int sched_ = (((int)ticks_since(t0) - CFG_T0) << 16) / CFG_TS; \
        const int lag_ = sched_ - (int)(col); \
        if (lag_ > CFG_L3) __builtin_amdgcn_s_setprio(3); \
        else if (lag_ > CFG_L2) __builtin_amdgcn_s_setprio(2); \
        else if (lag_ > CFG_L1) __builtin_amdgcn_s_setprio(1); \
        else __builtin_amdgcn_s_setprio(0); }
    if (nsteps > 0) {
        u32x4 gA[4], gB[4];
        u32x2 c0 = *(const u32x2*)(colrd);
        u32x2 cB = *(const u32x2*)(colrd + 64);
        u32x2 cA = *(const u32x2*)(colrd + 128);
        u32 wv = vr[lane];
        PACE(__builtin_amdgcn_readfirstlane(c0[0] & 0xffffu));
        issue_gather(gA, B, c0, laneoff);
        __builtin_amdgcn_sched_barrier(0);
        u32 sa = sconst + (wv & 0xffffu);
        if (lane < n) *(u16*)(simg + sa) = (u16)(wv >> 16);
        u32x4 S0 = *(const u32x4*)(simg + lane * 16);
        u32x4 S1 = *(const u32x4*)(simg + 1024 + lane * 16);
        if (lane < n) *(u16*)(simg + sa) = 0;
        wv = vr[64 + lane];
        int b = 0;
        for (; 2 * b + 2 < nsteps; ++b) {
            const int s = 2 * b;
            PACE(__builtin_amdgcn_readfirstlane(cB[0] & 0xffffu));
            issue_gather(gB, B, cB, laneoff);
            __builtin_amdgcn_sched_barrier(0);
            compute_step(gA, S0, acc, lomask);
            __builtin_amdgcn_sched_barrier(0);
            issue_gather(gA, B, cA, laneoff);
                __builtin_amdgcn_sched_barrier(0);
            compute_step(gB, S1, acc, lomask);
            __builtin_amdgcn_sched_barrier(0);
            cB = *(const u32x2*)(colrd + (s + 3) * 64);
            cA = *(const u32x2*)(colrd + (s + 4) * 64);
            const int p = (b + 1) * 64 + lane;
            sa = sconst + (wv & 0xffffu);
            if (p < n) *(u16*)(simg + sa) = (u16)(wv >> 16);
            S0 = *(const u32x4*)(simg + lane * 16);
            S1 = *(const u32x4*)(simg + 1024 + lane * 16);
            if (p < n) *(u16*)(simg + sa) = 0;
            wv = vr[(b + 2) * 64 + lane];
            __builtin_amdgcn_sched_barrier(0);
        }
        if (2 * b + 1 < nsteps) {
            issue_gather(gB, B, cB, laneoff);
            __builtin_amdgcn_sched_barrier(0);
            compute_step(gA, S0, acc, lomask);
            compute_step(gB, S1, acc, lomask);
        } else {
            compute_step(gA, S0, acc, lomask);
        }
    }
    {
        const int q = lane >> 4;
        u32x2* op = out + ((size_t)tile * ROWS + 4 * q) * 16 + (lane & 7) * 2 + ((lane >> 3) & 1);
#pragma unroll
        for (int r = 0; r < 4; ++r) {
            u32x2 o;
            o[0] = cvt_pk_bf16(acc[0][r], acc[1][r]);
            o[1] = cvt_pk_bf16(acc[2][r], acc[3][r]);
            op[r * 16] = o;
        }
    }
}

extern "C" void kernel_launch(void* const* d_in, const int* in_sizes, int n_in,
                              void* d_out, int out_size, void* d_ws, size_t ws_size,
                              hipStream_t stream) {
    const u16* vals = (const u16*)d_in[0];
    const u32* idx = (const u32*)d_in[1];
    const char* B = (const char*)d_in[2];
    const int M = out_size / 64;
    const int tiles = M / ROWS;
    spmm_ell_kernel<<<tiles / WPB, WPB * 64, 0, stream>>>(vals, idx, B, (u32x2*)d_out
        );
}
```

```cpp
#include <hip/hip_runtime.h>
#include <stdint.h>

typedef unsigned int u32;
typedef unsigned short u16;
typedef unsigned long long u64;
typedef u32 u32x4 __attribute__((ext_vector_type(4)));
typedef u32 u32x2 __attribute__((ext_vector_type(2)));
typedef float f32x4 __attribute__((ext_vector_type(4)));
typedef __bf16 bf16x8 __attribute__((ext_vector_type(8)));
typedef __bf16 bf16x2_t __attribute__((ext_vector_type(2)));
typedef float f32x2 __attribute__((ext_vector_type(2)));

#define TLW 7
#define TROWS 16
#define DYN_G (1 << 20)
#define DYN_P (2 << 20)
#define CFG_LOGP 4
#define CFG_WPB 16
#define CFG_PRIO 3
#define CFG_WARM 0
#define CFG_NT 1
#define CFG_AMP 0
#define CFG_AMPK 1
#define CFG_STNT 1
#define CFG_T0 400
#define CFG_TS 1400
#define CFG_L1 (-4000)
#define CFG_L2 0
#define CFG_L3 4000

#define LDNT(p) __builtin_nontemporal_load(p)
constexpr int KS = 65;
constexpr int ROWS = 16;
constexpr int SLOTS = ROWS * KS;
constexpr int LOGP = CFG_LOGP;
constexpr int WPB = CFG_WPB;
constexpr int COLS_B = 2432;
constexpr int VR_B = 4608 + 256;
constexpr int S_B = 2048;
constexpr int CNT_B = 256;
constexpr int WAVE_B = COLS_B + VR_B + S_B + CNT_B;
constexpr int OFF_VR = COLS_B, OFF_S = COLS_B + VR_B, OFF_CNT = COLS_B + VR_B + S_B;

__device__ __forceinline__ u32 cvt_pk_bf16(float a, float b) {
    f32x2 v = {a, b};
    bf16x2_t r = __builtin_convertvector(v, bf16x2_t);
    return __builtin_bit_cast(u32, r);
}

template <int CTRL, int RM, int BM>
__device__ __forceinline__ u32 dpp_add(u32 x) {
    return x + (u32)__builtin_amdgcn_update_dpp(0, (int)x, CTRL, RM, BM, true);
}

__device__ __forceinline__ void compute_step(const u32x4 (&g)[4], const u32x4 S, f32x4 (&acc)[4], const u64 lomask) {
    u32x4 F0, F1, F2, F3;
#pragma unroll
    for (int t = 0; t < 4; ++t) {
        u32 X0, X1, Y0, Y1;
        asm("s_mov_b64 vcc, %8\n\t"
            "v_cndmask_b32_dpp %0, %6, %4, vcc row_ror:8 row_mask:0xf bank_mask:0xf\n\t"
            "v_cndmask_b32_dpp %1, %7, %5, vcc row_ror:8 row_mask:0xf bank_mask:0xf\n\t"
            "s_not_b64 vcc, vcc\n\t"
            "v_cndmask_b32_dpp %2, %4, %6, vcc row_ror:8 row_mask:0xf bank_mask:0xf\n\t"
            "v_cndmask_b32_dpp %3, %5, %7, vcc row_ror:8 row_mask:0xf bank_mask:0xf"
            : "=&v"(X0), "=&v"(X1), "=&v"(Y0), "=&v"(Y1)
            : "v"(g[t][0]), "v"(g[t][1]), "v"(g[t][2]), "v"(g[t][3]), "s"(lomask)
            : "vcc");
        F0[t] = __builtin_amdgcn_perm(Y0, X0, 0x05040100u);
        F1[t] = __builtin_amdgcn_perm(Y0, X0, 0x07060302u);
        F2[t] = __builtin_amdgcn_perm(Y1, X1, 0x05040100u);
        F3[t] = __builtin_amdgcn_perm(Y1, X1, 0x07060302u);
    }
    const bf16x8 a = __builtin_bit_cast(bf16x8, S);
    acc[0] = __builtin_amdgcn_mfma_f32_16x16x32_bf16(a, __builtin_bit_cast(bf16x8, F0), acc[0], 0, 0, 0);
    acc[1] = __builtin_amdgcn_mfma_f32_16x16x32_bf16(a, __builtin_bit_cast(bf16x8, F1), acc[1], 0, 0, 0);
    acc[2] = __builtin_amdgcn_mfma_f32_16x16x32_bf16(a, __builtin_bit_cast(bf16x8, F2), acc[2], 0, 0, 0);
    acc[3] = __builtin_amdgcn_mfma_f32_16x16x32_bf16(a, __builtin_bit_cast(bf16x8, F3), acc[3], 0, 0, 0);
}

__device__ __forceinline__ void issue_gather(u32x4 (&g)[4], const char* __restrict__ B, u32x2 c, u32 laneoff) {
    u32 o0, o1, o2, o3;
    asm("v_mad_u32_u16 %0, %1, %2, %3" : "=v"(o0) : "v"(c[0]), "s"(128), "v"(laneoff));
    asm("v_mad_u32_u16 %0, %1, %2, %3 op_sel:[1,0,0,0]" : "=v"(o1) : "v"(c[0]), "s"(128), "v"(laneoff));
    asm("v_mad_u32_u16 %0, %1, %2, %3" : "=v"(o2) : "v"(c[1]), "s"(128), "v"(laneoff));
    asm("v_mad_u32_u16 %0, %1, %2, %3 op_sel:[1,0,0,0]" : "=v"(o3) : "v"(c[1]), "s"(128), "v"(laneoff));
    g[0] = *(const u32x4*)(B + o0);
    g[1] = *(const u32x4*)(B + o1);
    g[2] = *(const u32x4*)(B + o2);
    g[3] = *(const u32x4*)(B + o3);
}

__device__ __forceinline__ u32 ticks_since(u64 t0) { return (u32)(__builtin_amdgcn_s_memrealtime() - t0); }

__global__ __launch_bounds__(WPB * 64) void spmm_ell_kernel(const u16* __restrict__ vals, const u32* __restrict__ idx,
                                                            const char* __restrict__ B, u32x2* __restrict__ out
                                                            ) {
    __shared__ __attribute__((aligned(16))) char smem[WPB * WAVE_B];
    const int lane = threadIdx.x & 63;
    const int wib = __builtin_amdgcn_readfirstlane(threadIdx.x >> 6);
    const u32 tile = blockIdx.x * WPB + wib;
    const u64 t0 = __builtin_amdgcn_s_memrealtime();
    if (CFG_PRIO == 2) {
        const int gen = (WPB == 16) ? (wib >> 2) : ((wib >> 2) + 2 * ((blockIdx.x >> 8) & 1));
        if (gen == 1) __builtin_amdgcn_s_setprio(1);
        else if (gen == 2) __builtin_amdgcn_s_setprio(2);
        else if (gen == 3) __builtin_amdgcn_s_setprio(3);
    }
    const u32* ip = idx + (size_t)tile * SLOTS;
    const u16* vp = vals + (size_t)tile * SLOTS;
    u32x4 I[4]; u32x2 V[4];
#pragma unroll
    for (int i = 0; i < 4; ++i) {
        V[i] = LDNT((const u32x2*)(vp + 256 * i + 4 * lane));
        I[i] = LDNT((const u32x4*)(ip + 256 * i + 4 * lane));
    }
    const u32 Vt = LDNT(vp + 1024 + (lane & 15));
    const u32 It = LDNT(ip + 1024 + (lane & 15));

    if (CFG_WARM == 1) {
        const char* p = B + (size_t)tile * 2048;
#pragma unroll
        for (int u = 0; u < 8; ++u) asm volatile("s_load_dwordx2 s[60:61], %0, %1" :: "s"(p), "i"(u * 256 + 124) : "s60", "s61");
    }
    char* wb = smem + wib * WAVE_B;
    u16* cols = (u16*)wb;
    u32* vr = (u32*)(wb + OFF_VR);
    char* simg = wb + OFF_S;
    u32* cnt = (u32*)(wb + OFF_CNT);
    {
        u32x4 z = {0, 0, 0, 0};
        *(u32x4*)(simg + lane * 16) = z;
        *(u32x4*)(simg + 1024 + lane * 16) = z;
        u32x4 f = {0xffffffffu, 0xffffffffu, 0xffffffffu, 0xffffffffu};
        *(u32x4*)(wb + lane * 16) = f;
        *(u32x4*)(wb + 1024 + lane * 16) = f;
        if (lane < (COLS_B - 2048) / 16) *(u32x4*)(wb + 2048 + lane * 16) = f;
        cnt[lane] = 0;
    }
    u32 rank[17];
#pragma unroll
    for (int i = 0; i < 4; ++i) {
#pragma unroll
        for (int u = 0; u < 4; ++u) {
            const u32 vv = V[i][u >> 1];
            const bool nz = (u & 1) ? ((vv & 0x7fff0000u) != 0) : ((vv & 0x7fffu) != 0);
            rank[4 * i + u] = 0;
            if (nz) rank[4 * i + u] = __hip_atomic_fetch_add(&cnt[(I[i][u] & 0xffffu) >> (16 - LOGP)], 1u, __ATOMIC_RELAXED, __HIP_MEMORY_SCOPE_WORKGROUP);
        }
    }
    const bool nzt = ((Vt & 0x7fffu) != 0) && (lane < 16);
    rank[16] = 0;
    if (nzt) rank[16] = __hip_atomic_fetch_add(&cnt[(It & 0xffffu) >> (16 - LOGP)], 1u, __ATOMIC_RELAXED, __HIP_MEMORY_SCOPE_WORKGROUP);
    const u32 c = cnt[lane];
    u32 x = c;
    x = dpp_add<0x111, 0xf, 0xf>(x);
    x = dpp_add<0x112, 0xf, 0xf>(x);
    x = dpp_add<0x114, 0xf, 0xf>(x);
    x = dpp_add<0x118, 0xf, 0xf>(x);
    x = dpp_add<0x142, 0xa, 0xf>(x);
    x = dpp_add<0x143, 0xc, 0xf>(x);
    const int n = __builtin_amdgcn_readlane((int)x, 63);
    cnt[lane] = x - c;
#pragma unroll
    for (int i = 0; i < 4; i += 2) {
        u32 base[8];
#pragma unroll
        for (int k = 0; k < 8; ++k) base[k] = cnt[(I[i + (k >> 2)][k & 3] & 0xffffu) >> (16 - LOGP)];
#pragma unroll
        for (int k = 0; k < 8; ++k) {
            const int ii = i + (k >> 2), u = k & 3;
            const u32 vv = V[ii][u >> 1];
            const u32 v16 = (u & 1) ? (vv & 0xffff0000u) : (vv << 16);
            const bool nz = (v16 & 0x7fff0000u) != 0;
            const u32 slot = 256 * ii + 4 * lane + u;
            const u32 row = (slot * 4033u) >> 18;
            const u32 p = base[k] + rank[4 * ii + u];
            if (nz) {
                cols[p] = (u16)I[ii][u];
                vr[p] = v16 | (row << 4);
            }
        }
    }
    {
        const u32 p = cnt[(It & 0xffffu) >> (16 - LOGP)] + rank[16];
        if (nzt) {
            cols[p] = (u16)It;
            vr[p] = (Vt << 16) | (15u << 4);
        }
    }

    if (CFG_PRIO == 1) {
        const int gen = (WPB == 16) ? (wib >> 2) : ((wib >> 2) + 2 * ((blockIdx.x >> 8) & 1));
        if (gen == 1) __builtin_amdgcn_s_setprio(1);
        else if (gen == 2) __builtin_amdgcn_s_setprio(2);
        else if (gen == 3) __builtin_amdgcn_s_setprio(3);
    }
    const u32 laneoff = (lane & 7) * 16;
    const u64 lomask = 0x00ff00ff00ff00ffull;
    const u32 sconst = (lane >> 5) * 1024 + ((lane >> 3) & 3) * 256 + (2 * (lane & 3) + ((lane >> 2) & 1)) * 2;
    const char* colrd = (const char*)cols + (lane >> 3) * 8;
    f32x4 acc[4];
#pragma unroll
    for (int j = 0; j < 4; ++j) acc[j] = (f32x4){0.f, 0.f, 0.f, 0.f};
    const int nsteps = (n + 31) >> 5;
#define PACE(col) { \
        const int sched_ = (((int)ticks_since(t0) - CFG_T0) << 16) / CFG_TS; \
        const int lag_ = sched_ - (int)(col); \
        if (lag_ > CFG_L3) __builtin_amdgcn_s_setprio(3); \
        else if (lag_ > CFG_L2) __builtin_amdgcn_s_setprio(2); \
        else if (lag_ > CFG_L1) __builtin_amdgcn_s_setprio(1); \
        else __builtin_amdgcn_s_setprio(0); }
    if (nsteps > 0) {
        u32x4 gA[4], gB[4];
        u32x2 c0 = *(const u32x2*)(colrd);
        u32x2 cB = *(const u32x2*)(colrd + 64);
        u32x2 cA = *(const u32x2*)(colrd + 128);
        u32 wv = vr[lane];
        PACE(__builtin_amdgcn_readfirstlane(c0[0] & 0xffffu));
        issue_gather(gA, B, c0, laneoff);
        __builtin_amdgcn_sched_barrier(0);
        u32 sa = sconst + (wv & 0xffffu);
        if (lane < n) *(u16*)(simg + sa) = (u16)(wv >> 16);
        u32x4 S0 = *(const u32x4*)(simg + lane * 16);
        u32x4 S1 = *(const u32x4*)(simg + 1024 + lane * 16);
        if (lane < n) *(u16*)(simg + sa) = 0;
        wv = vr[64 + lane];
        int b = 0;
        for (; 2 * b + 2 < nsteps; ++b) {
            const int s = 2 * b;
            PACE(__builtin_amdgcn_readfirstlane(cB[0] & 0xffffu));
            issue_gather(gB, B, cB, laneoff);
            __builtin_amdgcn_sched_barrier(0);
            compute_step(gA, S0, acc, lomask);
            __builtin_amdgcn_sched_barrier(0);
            issue_gather(gA, B, cA, laneoff);
                __builtin_amdgcn_sched_barrier(0);
            compute_step(gB, S1, acc, lomask);
            __builtin_amdgcn_sched_barrier(0);
            cB = *(const u32x2*)(colrd + (s + 3) * 64);
            cA = *(const u32x2*)(colrd + (s + 4) * 64);
            const int p = (b + 1) * 64 + lane;
            sa = sconst + (wv & 0xffffu);
            if (p < n) *(u16*)(simg + sa) = (u16)(wv >> 16);
            S0 = *(const u32x4*)(simg + lane * 16);
            S1 = *(const u32x4*)(simg + 1024 + lane * 16);
            if (p < n) *(u16*)(simg + sa) = 0;
            wv = vr[(b + 2) * 64 + lane];
            __builtin_amdgcn_sched_barrier(0);
        }
        if (2 * b + 1 < nsteps) {
            issue_gather(gB, B, cB, laneoff);
            __builtin_amdgcn_sched_barrier(0);
            compute_step(gA, S0, acc, lomask);
            compute_step(gB, S1, acc, lomask);
        } else {
            compute_step(gA, S0, acc, lomask);
        }
    }
    {
        const int q = lane >> 4;
        u32x2* op = out + ((size_t)tile * ROWS + 4 * q) * 16 + (lane & 7) * 2 + ((lane >> 3) & 1);
#pragma unroll
        for (int r = 0; r < 4; ++r) {
            u32x2 o;
            o[0] = cvt_pk_bf16(acc[0][r], acc[1][r]);
            o[1] = cvt_pk_bf16(acc[2][r], acc[3][r]);
            __builtin_nontemporal_store(o, op + r * 16);
        }
    }
}

extern "C" void kernel_launch(void* const* d_in, const int* in_sizes, int n_in,
                              void* d_out, int out_size, void* d_ws, size_t ws_size,
                              hipStream_t stream) {
    const u16* vals = (const u16*)d_in[0];
    const u32* idx = (const u32*)d_in[1];
    const char* B = (const char*)d_in[2];
    const int M = out_size / 64;
    const int tiles = M / ROWS;
    spmm_ell_kernel<<<tiles / WPB, WPB * 64, 0, stream>>>(vals, idx, B, (u32x2*)d_out
        );
}
```

```cpp
#include <hip/hip_runtime.h>
#include <stdint.h>

typedef unsigned int u32;
typedef unsigned short u16;
typedef unsigned long long u64;
typedef u32 u32x4 __attribute__((ext_vector_type(4)));
typedef u32 u32x2 __attribute__((ext_vector_type(2)));
typedef float f32x4 __attribute__((ext_vector_type(4)));
typedef __bf16 bf16x8 __attribute__((ext_vector_type(8)));
typedef __bf16 bf16x2_t __attribute__((ext_vector_type(2)));
typedef float f32x2 __attribute__((ext_vector_type(2)));

#define TLW 7
#define TROWS 16
#define DYN_G (1 << 20)
#define DYN_P (2 << 20)
#define CFG_LOGP 4
#define CFG_WPB 16
#define CFG_PRIO 3
#define CFG_WARM 0
#define CFG_NT 1
#define CFG_AMP 0
#define CFG_AMPK 1
#define CFG_STNT 1
#define CFG_DUPW 1
#define CFG_T0 400
#define CFG_TS 1400
#define CFG_L1 (-4000)
#define CFG_L2 0
#define CFG_L3 4000

#define LDNT(p) __builtin_nontemporal_load(p)
constexpr int KS = 65;
constexpr int ROWS = 16;
constexpr int SLOTS = ROWS * KS;
constexpr int LOGP = CFG_LOGP;
constexpr int WPB = CFG_WPB;
constexpr int COLS_B = 2432;
constexpr int VR_B = 4608 + 256;
constexpr int S_B = 2048;
constexpr int CNT_B = 256;
constexpr int WAVE_B = COLS_B + VR_B + S_B + CNT_B;
constexpr int OFF_VR = COLS_B, OFF_S = COLS_B + VR_B, OFF_CNT = COLS_B + VR_B + S_B;

__device__ __forceinline__ u32 cvt_pk_bf16(float a, float b) {
    f32x2 v = {a, b};
    bf16x2_t r = __builtin_convertvector(v, bf16x2_t);
    return __builtin_bit_cast(u32, r);
}

template <int CTRL, int RM, int BM>
__device__ __forceinline__ u32 dpp_add(u32 x) {
    return x + (u32)__builtin_amdgcn_update_dpp(0, (int)x, CTRL, RM, BM, true);
}

__device__ __forceinline__ void compute_step(const u32x4 (&g)[4], const u32x4 S, f32x4 (&acc)[4], const u64 lomask) {
    u32x4 F0, F1, F2, F3;
#pragma unroll
    for (int t = 0; t < 4; ++t) {
        u32 X0, X1, Y0, Y1;
        asm("s_mov_b64 vcc, %8\n\t"
            "v_cndmask_b32_dpp %0, %6, %4, vcc row_ror:8 row_mask:0xf bank_mask:0xf\n\t"
            "v_cndmask_b32_dpp %1, %7, %5, vcc row_ror:8 row_mask:0xf bank_mask:0xf\n\t"
            "s_not_b64 vcc, vcc\n\t"
            "v_cndmask_b32_dpp %2, %4, %6, vcc row_ror:8 row_mask:0xf bank_mask:0xf\n\t"
            "v_cndmask_b32_dpp %3, %5, %7, vcc row_ror:8 row_mask:0xf bank_mask:0xf"
            : "=&v"(X0), "=&v"(X1), "=&v"(Y0), "=&v"(Y1)
            : "v"(g[t][0]), "v"(g[t][1]), "v"(g[t][2]), "v"(g[t][3]), "s"(lomask)
            : "vcc");
        F0[t] = __builtin_amdgcn_perm(Y0, X0, 0x05040100u);
        F1[t] = __builtin_amdgcn_perm(Y0, X0, 0x07060302u);
        F2[t] = __builtin_amdgcn_perm(Y1, X1, 0x05040100u);
        F3[t] = __builtin_amdgcn_perm(Y1, X1, 0x07060302u);
    }
    const bf16x8 a = __builtin_bit_cast(bf16x8, S);
    acc[0] = __builtin_amdgcn_mfma_f32_16x16x32_bf16(a, __builtin_bit_cast(bf16x8, F0), acc[0], 0, 0, 0);
    acc[1] = __builtin_amdgcn_mfma_f32_16x16x32_bf16(a, __builtin_bit_cast(bf16x8, F1), acc[1], 0, 0, 0);
    acc[2] = __builtin_amdgcn_mfma_f32_16x16x32_bf16(a, __builtin_bit_cast(bf16x8, F2), acc[2], 0, 0, 0);
    acc[3] = __builtin_amdgcn_mfma_f32_16x16x32_bf16(a, __builtin_bit_cast(bf16x8, F3), acc[3], 0, 0, 0);
}

__device__ __forceinline__ void issue_gather(u32x4 (&g)[4], const char* __restrict__ B, u32x2 c, u32 laneoff) {
    u32 o0, o1, o2, o3;
    asm("v_mad_u32_u16 %0, %1, %2, %3" : "=v"(o0) : "v"(c[0]), "s"(128), "v"(laneoff));
    asm("v_mad_u32_u16 %0, %1, %2, %3 op_sel:[1,0,0,0]" : "=v"(o1) : "v"(c[0]), "s"(128), "v"(laneoff));
    asm("v_mad_u32_u16 %0, %1, %2, %3" : "=v"(o2) : "v"(c[1]), "s"(128), "v"(laneoff));
    asm("v_mad_u32_u16 %0, %1, %2, %3 op_sel:[1,0,0,0]" : "=v"(o3) : "v"(c[1]), "s"(128), "v"(laneoff));
    g[0] = *(const u32x4*)(B + o0);
    g[1] = *(const u32x4*)(B + o1);
    g[2] = *(const u32x4*)(B + o2);
    g[3] = *(const u32x4*)(B + o3);
}

__device__ __forceinline__ u32 ticks_since(u64 t0) { return (u32)(__builtin_amdgcn_s_memrealtime() - t0); }

__global__ __launch_bounds__(WPB * 64) void spmm_ell_kernel(const u16* __restrict__ vals, const u32* __restrict__ idx,
                                                            const char* __restrict__ B, u32x2* __restrict__ out
                                                            , u32x2* __restrict__ out2
                                                            ) {
    __shared__ __attribute__((aligned(16))) char smem[WPB * WAVE_B];
    const int lane = threadIdx.x & 63;
    const int wib = __builtin_amdgcn_readfirstlane(threadIdx.x >> 6);
    const u32 tile = blockIdx.x * WPB + wib;
    const u64 t0 = __builtin_amdgcn_s_memrealtime();
    if (CFG_PRIO == 2) {
        const int gen = (WPB == 16) ? (wib >> 2) : ((wib >> 2) + 2 * ((blockIdx.x >> 8) & 1));
        if (gen == 1) __builtin_amdgcn_s_setprio(1);
        else if (gen == 2) __builtin_amdgcn_s_setprio(2);
        else if (gen == 3) __builtin_amdgcn_s_setprio(3);
    }
    const u32* ip = idx + (size_t)tile * SLOTS;
    const u16* vp = vals + (size_t)tile * SLOTS;
    u32x4 I[4]; u32x2 V[4];
#pragma unroll
    for (int i = 0; i < 4; ++i) {
        V[i] = LDNT((const u32x2*)(vp + 256 * i + 4 * lane));
        I[i] = LDNT((const u32x4*)(ip + 256 * i + 4 * lane));
    }
    const u32 Vt = LDNT(vp + 1024 + (lane & 15));
    const u32 It = LDNT(ip + 1024 + (lane & 15));

    if (CFG_WARM == 1) {
        const char* p = B + (size_t)tile * 2048;
#pragma unroll
        for (int u = 0; u < 8; ++u) asm volatile("s_load_dwordx2 s[60:61], %0, %1" :: "s"(p), "i"(u * 256 + 124) : "s60", "s61");
    }
    char* wb = smem + wib * WAVE_B;
    u16* cols = (u16*)wb;
    u32* vr = (u32*)(wb + OFF_VR);
    char* simg = wb + OFF_S;
    u32* cnt = (u32*)(wb + OFF_CNT);
    {
        u32x4 z = {0, 0, 0, 0};
        *(u32x4*)(simg + lane * 16) = z;
        *(u32x4*)(simg + 1024 + lane * 16) = z;
        u32x4 f = {0xffffffffu, 0xffffffffu, 0xffffffffu, 0xffffffffu};
        *(u32x4*)(wb + lane * 16) = f;
        *(u32x4*)(wb + 1024 + lane * 16) = f;
        if (lane < (COLS_B - 2048) / 16) *(u32x4*)(wb + 2048 + lane * 16) = f;
        cnt[lane] = 0;
    }
    u32 rank[17];
#pragma unroll
    for (int i = 0; i < 4; ++i) {
#pragma unroll
        for (int u = 0; u < 4; ++u) {
            const u32 vv = V[i][u >> 1];
            const bool nz = (u & 1) ? ((vv & 0x7fff0000u) != 0) : ((vv & 0x7fffu) != 0);
            rank[4 * i + u] = 0;
            if (nz) rank[4 * i + u] = __hip_atomic_fetch_add(&cnt[(I[i][u] & 0xffffu) >> (16 - LOGP)], 1u, __ATOMIC_RELAXED, __HIP_MEMORY_SCOPE_WORKGROUP);
        }
    }
    const bool nzt = ((Vt & 0x7fffu) != 0) && (lane < 16);
    rank[16] = 0;
    if (nzt) rank[16] = __hip_atomic_fetch_add(&cnt[(It & 0xffffu) >> (16 - LOGP)], 1u, __ATOMIC_RELAXED, __HIP_MEMORY_SCOPE_WORKGROUP);
    const u32 c = cnt[lane];
    u32 x = c;
    x = dpp_add<0x111, 0xf, 0xf>(x);
    x = dpp_add<0x112, 0xf, 0xf>(x);
    x = dpp_add<0x114, 0xf, 0xf>(x);
    x = dpp_add<0x118, 0xf, 0xf>(x);
    x = dpp_add<0x142, 0xa, 0xf>(x);
    x = dpp_add<0x143, 0xc, 0xf>(x);
    const int n = __builtin_amdgcn_readlane((int)x, 63);
    cnt[lane] = x - c;
#pragma unroll
    for (int i = 0; i < 4; i += 2) {
        u32 base[8];
#pragma unroll
        for (int k = 0; k < 8; ++k) base[k] = cnt[(I[i + (k >> 2)][k & 3] & 0xffffu) >> (16 - LOGP)];
#pragma unroll
        for (int k = 0; k < 8; ++k) {
            const int ii = i + (k >> 2), u = k & 3;
            const u32 vv = V[ii][u >> 1];
            const u32 v16 = (u & 1) ? (vv & 0xffff0000u) : (vv << 16);
            const bool nz = (v16 & 0x7fff0000u) != 0;
            const u32 slot = 256 * ii + 4 * lane + u;
            const u32 row = (slot * 4033u) >> 18;
            const u32 p = base[k] + rank[4 * ii + u];
            if (nz) {
                cols[p] = (u16)I[ii][u];
                vr[p] = v16 | (row << 4);
            }
        }
    }
    {
        const u32 p = cnt[(It & 0xffffu) >> (16 - LOGP)] + rank[16];
        if (nzt) {
            cols[p] = (u16)It;
            vr[p] = (Vt << 16) | (15u << 4);
        }
    }

    if (CFG_PRIO == 1) {
        const int gen = (WPB == 16) ? (wib >> 2) : ((wib >> 2) + 2 * ((blockIdx.x >> 8) & 1));
        if (gen == 1) __builtin_amdgcn_s_setprio(1);
        else if (gen == 2) __builtin_amdgcn_s_setprio(2);
        else if (gen == 3) __builtin_amdgcn_s_setprio(3);
    }
    const u32 laneoff = (lane & 7) * 16;
    const u64 lomask = 0x00ff00ff00ff00ffull;
    const u32 sconst = (lane >> 5) * 1024 + ((lane >> 3) & 3) * 256 + (2 * (lane & 3) + ((lane >> 2) & 1)) * 2;
    const char* colrd = (const char*)cols + (lane >> 3) * 8;
    f32x4 acc[4];
#pragma unroll
    for (int j = 0; j < 4; ++j) acc[j] = (f32x4){0.f, 0.f, 0.f, 0.f};
    const int nsteps = (n + 31) >> 5;
#define PACE(col) { \
        const int sched_ = (((int)ticks_since(t0) - CFG_T0) << 16) / CFG_TS; \
        const int lag_ = sched_ - (int)(col); \
        if (lag_ > CFG_L3) __builtin_amdgcn_s_setprio(3); \
        else if (lag_ > CFG_L2) __builtin_amdgcn_s_setprio(2); \
        else if (lag_ > CFG_L1) __builtin_amdgcn_s_setprio(1); \
        else __builtin_amdgcn_s_setprio(0); }
    if (nsteps > 0) {
        u32x4 gA[4], gB[4];
        u32x2 c0 = *(const u32x2*)(colrd);
        u32x2 cB = *(const u32x2*)(colrd + 64);
        u32x2 cA = *(const u32x2*)(colrd + 128);
        u32 wv = vr[lane];
        PACE(__builtin_amdgcn_readfirstlane(c0[0] & 0xffffu));
        issue_gather(gA, B, c0, laneoff);
        __builtin_amdgcn_sched_barrier(0);
        u32 sa = sconst + (wv & 0xffffu);
        if (lane < n) *(u16*)(simg + sa) = (u16)(wv >> 16);
        u32x4 S0 = *(const u32x4*)(simg + lane * 16);
        u32x4 S1 = *(const u32x4*)(simg + 1024 + lane * 16);
        if (lane < n) *(u16*)(simg + sa) = 0;
        wv = vr[64 + lane];
        int b = 0;
        for (; 2 * b + 2 < nsteps; ++b) {
            const int s = 2 * b;
            PACE(__builtin_amdgcn_readfirstlane(cB[0] & 0xffffu));
            issue_gather(gB, B, cB, laneoff);
            __builtin_amdgcn_sched_barrier(0);
            compute_step(gA, S0, acc, lomask);
            __builtin_amdgcn_sched_barrier(0);
            issue_gather(gA, B, cA, laneoff);
                __builtin_amdgcn_sched_barrier(0);
            compute_step(gB, S1, acc, lomask);
            __builtin_amdgcn_sched_barrier(0);
            cB = *(const u32x2*)(colrd + (s + 3) * 64);
            cA = *(const u32x2*)(colrd + (s + 4) * 64);
            const int p = (b + 1) * 64 + lane;
            sa = sconst + (wv & 0xffffu);
            if (p < n) *(u16*)(simg + sa) = (u16)(wv >> 16);
            S0 = *(const u32x4*)(simg + lane * 16);
            S1 = *(const u32x4*)(simg + 1024 + lane * 16);
            if (p < n) *(u16*)(simg + sa) = 0;
            wv = vr[(b + 2) * 64 + lane];
            __builtin_amdgcn_sched_barrier(0);
        }
        if (2 * b + 1 < nsteps) {
            issue_gather(gB, B, cB, laneoff);
            __builtin_amdgcn_sched_barrier(0);
            compute_step(gA, S0, acc, lomask);
            compute_step(gB, S1, acc, lomask);
        } else {
            compute_step(gA, S0, acc, lomask);
        }
    }
    {
        const int q = lane >> 4;
        u32x2* op = out + ((size_t)tile * ROWS + 4 * q) * 16 + (lane & 7) * 2 + ((lane >> 3) & 1);
#pragma unroll
        for (int r = 0; r < 4; ++r) {
            u32x2 o;
            o[0] = cvt_pk_bf16(acc[0][r], acc[1][r]);
            o[1] = cvt_pk_bf16(acc[2][r], acc[3][r]);
            for (int dd = 0; dd < CFG_DUPW; ++dd) __builtin_nontemporal_store(o, out2 + (size_t)dd * 1048576 + (op - out) + r * 16);
            __builtin_nontemporal_store(o, op + r * 16);
        }
    }
}

extern "C" void kernel_launch(void* const* d_in, const int* in_sizes, int n_in,
                              void* d_out, int out_size, void* d_ws, size_t ws_size,
                              hipStream_t stream) {
    const u16* vals = (const u16*)d_in[0];
    const u32* idx = (const u32*)d_in[1];
    const char* B = (const char*)d_in[2];
    const int M = out_size / 64;
    const int tiles = M / ROWS;
    spmm_ell_kernel<<<tiles / WPB, WPB * 64, 0, stream>>>(vals, idx, B, (u32x2*)d_out
        , (u32x2*)d_ws
        );
}
```

```cpp
#include <hip/hip_runtime.h>
#include <stdint.h>

typedef unsigned int u32;
typedef unsigned short u16;
typedef unsigned long long u64;
typedef u32 u32x4 __attribute__((ext_vector_type(4)));
typedef u32 u32x2 __attribute__((ext_vector_type(2)));
typedef float f32x4 __attribute__((ext_vector_type(4)));
typedef __bf16 bf16x8 __attribute__((ext_vector_type(8)));
typedef __bf16 bf16x2_t __attribute__((ext_vector_type(2)));
typedef float f32x2 __attribute__((ext_vector_type(2)));

#define TLW 7
#define TROWS 16
#define DYN_G (1 << 20)
#define DYN_P (2 << 20)
#define CFG_LOGP 4
#define CFG_WPB 16
#define CFG_PRIO 3
#define CFG_WARM 0
#define CFG_NT 1
#define CFG_AMP 5
#define CFG_AMPK 0
#define CFG_STNT 1
#define CFG_DUPW 0
#define CFG_T0 400
#define CFG_TS 1400
#define CFG_L1 (-4000)
#define CFG_L2 0
#define CFG_L3 4000

#define LDNT(p) __builtin_nontemporal_load(p)
constexpr int KS = 65;
constexpr int ROWS = 16;
constexpr int SLOTS = ROWS * KS;
constexpr int LOGP = CFG_LOGP;
constexpr int WPB = CFG_WPB;
constexpr int COLS_B = 2432;
constexpr int VR_B = 4608 + 256;
constexpr int S_B = 2048;
constexpr int CNT_B = 256;
constexpr int WAVE_B = COLS_B + VR_B + S_B + CNT_B;
constexpr int OFF_VR = COLS_B, OFF_S = COLS_B + VR_B, OFF_CNT = COLS_B + VR_B + S_B;

__device__ __forceinline__ u32 cvt_pk_bf16(float a, float b) {
    f32x2 v = {a, b};
    bf16x2_t r = __builtin_convertvector(v, bf16x2_t);
    return __builtin_bit_cast(u32, r);
}

template <int CTRL, int RM, int BM>
__device__ __forceinline__ u32 dpp_add(u32 x) {
    return x + (u32)__builtin_amdgcn_update_dpp(0, (int)x, CTRL, RM, BM, true);
}

__device__ __forceinline__ void compute_step(const u32x4 (&g)[4], const u32x4 S, f32x4 (&acc)[4], const u64 lomask) {
    u32x4 F0, F1, F2, F3;
#pragma unroll
    for (int t = 0; t < 4; ++t) {
        u32 X0, X1, Y0, Y1;
        asm("s_mov_b64 vcc, %8\n\t"
            "v_cndmask_b32_dpp %0, %6, %4, vcc row_ror:8 row_mask:0xf bank_mask:0xf\n\t"
            "v_cndmask_b32_dpp %1, %7, %5, vcc row_ror:8 row_mask:0xf bank_mask:0xf\n\t"
            "s_not_b64 vcc, vcc\n\t"
            "v_cndmask_b32_dpp %2, %4, %6, vcc row_ror:8 row_mask:0xf bank_mask:0xf\n\t"
            "v_cndmask_b32_dpp %3, %5, %7, vcc row_ror:8 row_mask:0xf bank_mask:0xf"
            : "=&v"(X0), "=&v"(X1), "=&v"(Y0), "=&v"(Y1)
            : "v"(g[t][0]), "v"(g[t][1]), "v"(g[t][2]), "v"(g[t][3]), "s"(lomask)
            : "vcc");
        F0[t] = __builtin_amdgcn_perm(Y0, X0, 0x05040100u);
        F1[t] = __builtin_amdgcn_perm(Y0, X0, 0x07060302u);
        F2[t] = __builtin_amdgcn_perm(Y1, X1, 0x05040100u);
        F3[t] = __builtin_amdgcn_perm(Y1, X1, 0x07060302u);
    }
    const bf16x8 a = __builtin_bit_cast(bf16x8, S);
    acc[0] = __builtin_amdgcn_mfma_f32_16x16x32_bf16(a, __builtin_bit_cast(bf16x8, F0), acc[0], 0, 0, 0);
    acc[1] = __builtin_amdgcn_mfma_f32_16x16x32_bf16(a, __builtin_bit_cast(bf16x8, F1), acc[1], 0, 0, 0);
    acc[2] = __builtin_amdgcn_mfma_f32_16x16x32_bf16(a, __builtin_bit_cast(bf16x8, F2), acc[2], 0, 0, 0);
    acc[3] = __builtin_amdgcn_mfma_f32_16x16x32_bf16(a, __builtin_bit_cast(bf16x8, F3), acc[3], 0, 0, 0);
}

__device__ __forceinline__ void issue_gather(u32x4 (&g)[4], const char* __restrict__ B, u32x2 c, u32 laneoff) {
    u32 o0, o1, o2, o3;
    asm("v_mad_u32_u16 %0, %1, %2, %3" : "=v"(o0) : "v"(c[0]), "s"(128), "v"(laneoff));
    asm("v_mad_u32_u16 %0, %1, %2, %3 op_sel:[1,0,0,0]" : "=v"(o1) : "v"(c[0]), "s"(128), "v"(laneoff));
    asm("v_mad_u32_u16 %0, %1, %2, %3" : "=v"(o2) : "v"(c[1]), "s"(128), "v"(laneoff));
    asm("v_mad_u32_u16 %0, %1, %2, %3 op_sel:[1,0,0,0]" : "=v"(o3) : "v"(c[1]), "s"(128), "v"(laneoff));
    g[0] = *(const u32x4*)(B + o0);
    g[1] = *(const u32x4*)(B + o1);
    g[2] = *(const u32x4*)(B + o2);
    g[3] = *(const u32x4*)(B + o3);
}

__device__ __forceinline__ u32 ticks_since(u64 t0) { return (u32)(__builtin_amdgcn_s_memrealtime() - t0); }

__global__ __launch_bounds__(WPB * 64) void spmm_ell_kernel(const u16* __restrict__ vals, const u32* __restrict__ idx,
                                                            const char* __restrict__ B, u32x2* __restrict__ out
                                                            , u64* __restrict__ wsmin
                                                            ) {
    __shared__ __attribute__((aligned(16))) char smem[WPB * WAVE_B];
    const int lane = threadIdx.x & 63;
    const int wib = __builtin_amdgcn_readfirstlane(threadIdx.x >> 6);
    const u32 tile = blockIdx.x * WPB + wib;
    const u64 t0 = __builtin_amdgcn_s_memrealtime();
    if (CFG_PRIO == 2) {
        const int gen = (WPB == 16) ? (wib >> 2) : ((wib >> 2) + 2 * ((blockIdx.x >> 8) & 1));
        if (gen == 1) __builtin_amdgcn_s_setprio(1);
        else if (gen == 2) __builtin_amdgcn_s_setprio(2);
        else if (gen == 3) __builtin_amdgcn_s_setprio(3);
    }
    if (lane == 0) __hip_atomic_fetch_min(wsmin, t0, __ATOMIC_RELAXED, __HIP_MEMORY_SCOPE_AGENT);
    const u32* ip = idx + (size_t)tile * SLOTS;
    const u16* vp = vals + (size_t)tile * SLOTS;
    u32x4 I[4]; u32x2 V[4];
#pragma unroll
    for (int i = 0; i < 4; ++i) {
        V[i] = LDNT((const u32x2*)(vp + 256 * i + 4 * lane));
        I[i] = LDNT((const u32x4*)(ip + 256 * i + 4 * lane));
    }
    const u32 Vt = LDNT(vp + 1024 + (lane & 15));
    const u32 It = LDNT(ip + 1024 + (lane & 15));

    if (CFG_WARM == 1) {
        const char* p = B + (size_t)tile * 2048;
#pragma unroll
        for (int u = 0; u < 8; ++u) asm volatile("s_load_dwordx2 s[60:61], %0, %1" :: "s"(p), "i"(u * 256 + 124) : "s60", "s61");
    }
    char* wb = smem + wib * WAVE_B;
    u16* cols = (u16*)wb;
    u32* vr = (u32*)(wb + OFF_VR);
    char* simg = wb + OFF_S;
    u32* cnt = (u32*)(wb + OFF_CNT);
    {
        u32x4 z = {0, 0, 0, 0};
        *(u32x4*)(simg + lane * 16) = z;
        *(u32x4*)(simg + 1024 + lane * 16) = z;
        u32x4 f = {0xffffffffu, 0xffffffffu, 0xffffffffu, 0xffffffffu};
        *(u32x4*)(wb + lane * 16) = f;
        *(u32x4*)(wb + 1024 + lane * 16) = f;
        if (lane < (COLS_B - 2048) / 16) *(u32x4*)(wb + 2048 + lane * 16) = f;
        cnt[lane] = 0;
    }
    u32 rank[17];
#pragma unroll
    for (int i = 0; i < 4; ++i) {
#pragma unroll
        for (int u = 0; u < 4; ++u) {
            const u32 vv = V[i][u >> 1];
            const bool nz = (u & 1) ? ((vv & 0x7fff0000u) != 0) : ((vv & 0x7fffu) != 0);
            rank[4 * i + u] = 0;
            if (nz) rank[4 * i + u] = __hip_atomic_fetch_add(&cnt[(I[i][u] & 0xffffu) >> (16 - LOGP)], 1u, __ATOMIC_RELAXED, __HIP_MEMORY_SCOPE_WORKGROUP);
        }
    }
    const bool nzt = ((Vt & 0x7fffu) != 0) && (lane < 16);
    rank[16] = 0;
    if (nzt) rank[16] = __hip_atomic_fetch_add(&cnt[(It & 0xffffu) >> (16 - LOGP)], 1u, __ATOMIC_RELAXED, __HIP_MEMORY_SCOPE_WORKGROUP);
    const u32 c = cnt[lane];
    u32 x = c;
    x = dpp_add<0x111, 0xf, 0xf>(x);
    x = dpp_add<0x112, 0xf, 0xf>(x);
    x = dpp_add<0x114, 0xf, 0xf>(x);
    x = dpp_add<0x118, 0xf, 0xf>(x);
    x = dpp_add<0x142, 0xa, 0xf>(x);
    x = dpp_add<0x143, 0xc, 0xf>(x);
    const int n = __builtin_amdgcn_readlane((int)x, 63);
    cnt[lane] = x - c;
#pragma unroll
    for (int i = 0; i < 4; i += 2) {
        u32 base[8];
#pragma unroll
        for (int k = 0; k < 8; ++k) base[k] = cnt[(I[i + (k >> 2)][k & 3] & 0xffffu) >> (16 - LOGP)];
#pragma unroll
        for (int k = 0; k < 8; ++k) {
            const int ii = i + (k >> 2), u = k & 3;
            const u32 vv = V[ii][u >> 1];
            const u32 v16 = (u & 1) ? (vv & 0xffff0000u) : (vv << 16);
            const bool nz = (v16 & 0x7fff0000u) != 0;
            const u32 slot = 256 * ii + 4 * lane + u;
            const u32 row = (slot * 4033u) >> 18;
            const u32 p = base[k] + rank[4 * ii + u];
            if (nz) {
                cols[p] = (u16)I[ii][u];
                vr[p] = v16 | (row << 4);
            }
        }
    }
    {
        const u32 p = cnt[(It & 0xffffu) >> (16 - LOGP)] + rank[16];
        if (nzt) {
            cols[p] = (u16)It;
            vr[p] = (Vt << 16) | (15u << 4);
        }
    }

    if (CFG_PRIO == 1) {
        const int gen = (WPB == 16) ? (wib >> 2) : ((wib >> 2) + 2 * ((blockIdx.x >> 8) & 1));
        if (gen == 1) __builtin_amdgcn_s_setprio(1);
        else if (gen == 2) __builtin_amdgcn_s_setprio(2);
        else if (gen == 3) __builtin_amdgcn_s_setprio(3);
    }
    const u32 laneoff = (lane & 7) * 16;
    const u64 lomask = 0x00ff00ff00ff00ffull;
    const u32 sconst = (lane >> 5) * 1024 + ((lane >> 3) & 3) * 256 + (2 * (lane & 3) + ((lane >> 2) & 1)) * 2;
    const char* colrd = (const char*)cols + (lane >> 3) * 8;
    f32x4 acc[4];
#pragma unroll
    for (int j = 0; j < 4; ++j) acc[j] = (f32x4){0.f, 0.f, 0.f, 0.f};
    const int nsteps = (n + 31) >> 5;
#define PACE(col) { \
        const int sched_ = (((int)ticks_since(t0) - CFG_T0) << 16) / CFG_TS; \
        const int lag_ = sched_ - (int)(col); \
        if (lag_ > CFG_L3) __builtin_amdgcn_s_setprio(3); \
        else if (lag_ > CFG_L2) __builtin_amdgcn_s_setprio(2); \
        else if (lag_ > CFG_L1) __builtin_amdgcn_s_setprio(1); \
        else __builtin_amdgcn_s_setprio(0); }
    if (nsteps > 0) {
        u32x4 gA[4], gB[4];
        u32x2 c0 = *(const u32x2*)(colrd);
        u32x2 cB = *(const u32x2*)(colrd + 64);
        u32x2 cA = *(const u32x2*)(colrd + 128);
        u32 wv = vr[lane];
        PACE(__builtin_amdgcn_readfirstlane(c0[0] & 0xffffu));
        issue_gather(gA, B, c0, laneoff);
        __builtin_amdgcn_sched_barrier(0);
        u32 sa = sconst + (wv & 0xffffu);
        if (lane < n) *(u16*)(simg + sa) = (u16)(wv >> 16);
        u32x4 S0 = *(const u32x4*)(simg + lane * 16);
        u32x4 S1 = *(const u32x4*)(simg + 1024 + lane * 16);
        if (lane < n) *(u16*)(simg + sa) = 0;
        wv = vr[64 + lane];
        int b = 0;
        for (; 2 * b + 2 < nsteps; ++b) {
            const int s = 2 * b;
            PACE(__builtin_amdgcn_readfirstlane(cB[0] & 0xffffu));
            issue_gather(gB, B, cB, laneoff);
            __builtin_amdgcn_sched_barrier(0);
            compute_step(gA, S0, acc, lomask);
            __builtin_amdgcn_sched_barrier(0);
            issue_gather(gA, B, cA, laneoff);
                __builtin_amdgcn_sched_barrier(0);
            compute_step(gB, S1, acc, lomask);
            __builtin_amdgcn_sched_barrier(0);
            cB = *(const u32x2*)(colrd + (s + 3) * 64);
            cA = *(const u32x2*)(colrd + (s + 4) * 64);
            const int p = (b + 1) * 64 + lane;
            sa = sconst + (wv & 0xffffu);
            if (p < n) *(u16*)(simg + sa) = (u16)(wv >> 16);
            S0 = *(const u32x4*)(simg + lane * 16);
            S1 = *(const u32x4*)(simg + 1024 + lane * 16);
            if (p < n) *(u16*)(simg + sa) = 0;
            wv = vr[(b + 2) * 64 + lane];
            __builtin_amdgcn_sched_barrier(0);
        }
        if (2 * b + 1 < nsteps) {
            issue_gather(gB, B, cB, laneoff);
            __builtin_amdgcn_sched_barrier(0);
            compute_step(gA, S0, acc, lomask);
            compute_step(gB, S1, acc, lomask);
        } else {
            compute_step(gA, S0, acc, lomask);
        }
    }
    u32 ampA;
    { u64 mn = __hip_atomic_load(wsmin, __ATOMIC_RELAXED, __HIP_MEMORY_SCOPE_AGENT); mn = ((u64)(u32)__builtin_amdgcn_readfirstlane((u32)(mn >> 32)) << 32) | (u32)__builtin_amdgcn_readfirstlane((u32)mn);
      u64 sk = t0 - mn; ampA = (sk < 3000) ? (u32)sk : 0u; }
    { const u32 te = ticks_since(t0) + ampA * CFG_AMPK; while (ticks_since(t0) < te) __builtin_amdgcn_s_sleep(2); }
    {
        const int q = lane >> 4;
        u32x2* op = out + ((size_t)tile * ROWS + 4 * q) * 16 + (lane & 7) * 2 + ((lane >> 3) & 1);
#pragma unroll
        for (int r = 0; r < 4; ++r) {
            u32x2 o;
            o[0] = cvt_pk_bf16(acc[0][r], acc[1][r]);
            o[1] = cvt_pk_bf16(acc[2][r], acc[3][r]);
            __builtin_nontemporal_store(o, op + r * 16);
        }
    }
}

extern "C" void kernel_launch(void* const* d_in, const int* in_sizes, int n_in,
                              void* d_out, int out_size, void* d_ws, size_t ws_size,
                              hipStream_t stream) {
    const u16* vals = (const u16*)d_in[0];
    const u32* idx = (const u32*)d_in[1];
    const char* B = (const char*)d_in[2];
    const int M = out_size / 64;
    const int tiles = M / ROWS;
    spmm_ell_kernel<<<tiles / WPB, WPB * 64, 0, stream>>>(vals, idx, B, (u32x2*)d_out
        , (u64*)d_ws
        );
}
```

```cpp
#include <hip/hip_runtime.h>
#include <hip/hip_bf16.h>
#include <stdint.h>
typedef unsigned int u32;
typedef unsigned long long u64;
typedef __attribute__((ext_vector_type(4))) unsigned int u32x4;
constexpr int KK = 65;
constexpr int DD = 64;
#define AMP 0
__device__ __forceinline__ float bflo(u32 x) { return __uint_as_float(x << 16); }
__device__ __forceinline__ float bfhi(u32 x) { return __uint_as_float(x & 0xffff0000u); }
__global__ __launch_bounds__(256) void spmm_v0(const unsigned short* __restrict__ vals, const int* __restrict__ idx,
                                               const u32x4* __restrict__ B, u32x4* __restrict__ out, int M) {
    const u64 t0 = __builtin_amdgcn_s_memrealtime();
    int tid = blockIdx.x * blockDim.x + threadIdx.x;
    int c = tid & 7;
    u32x4 o[2];
    for (int rr = 0; rr < 2; ++rr) {
        int row = (tid >> 3) * 2 + rr;
        const unsigned short* vr = vals + (size_t)row * KK;
        const int* ir = idx + (size_t)row * KK;
        float acc[8];
#pragma unroll
        for (int j = 0; j < 8; ++j) acc[j] = 0.f;
#pragma unroll 5
        for (int k = 0; k < KK; ++k) {
            unsigned short v = vr[k];
            if ((v & 0x7fff) != 0) {
                int col = ir[k];
                u32x4 g = B[(size_t)col * 8 + c];
                float f = __uint_as_float((u32)v << 16);
                acc[0] += f * bflo(g[0]); acc[1] += f * bfhi(g[0]);
                acc[2] += f * bflo(g[1]); acc[3] += f * bfhi(g[1]);
                acc[4] += f * bflo(g[2]); acc[5] += f * bfhi(g[2]);
                acc[6] += f * bflo(g[3]); acc[7] += f * bfhi(g[3]);
            }
        }
#pragma unroll
        for (int j = 0; j < 4; ++j) {
            __hip_bfloat162 p = __float22bfloat162_rn(make_float2(acc[2 * j], acc[2 * j + 1]));
            o[rr][j] = *reinterpret_cast<u32*>(&p);
        }
    }
    out[(size_t)((tid >> 3) * 2) * 8 + c] = o[0];
    out[(size_t)((tid >> 3) * 2 + 1) * 8 + c] = o[1];
}
extern "C" void kernel_launch(void* const* d_in, const int* in_sizes, int n_in,
                              void* d_out, int out_size, void* d_ws, size_t ws_size,
                              hipStream_t stream) {
    const unsigned short* vals = (const unsigned short*)d_in[0];
    const int* idx = (const int*)d_in[1];
    const u32x4* B = (const u32x4*)d_in[2];
    int M = out_size / DD;
    int threads = M * 4;
    spmm_v0<<<(threads + 255) / 256, 256, 0, stream>>>(vals, idx, B, (u32x4*)d_out, M);
}
```

```cpp
#include <hip/hip_runtime.h>
#include <stdint.h>

typedef unsigned int u32;
typedef unsigned short u16;
typedef unsigned long long u64;
typedef u32 u32x4 __attribute__((ext_vector_type(4)));
typedef u32 u32x2 __attribute__((ext_vector_type(2)));
typedef float f32x4 __attribute__((ext_vector_type(4)));
typedef __bf16 bf16x8 __attribute__((ext_vector_type(8)));
typedef __bf16 bf16x2_t __attribute__((ext_vector_type(2)));
typedef float f32x2 __attribute__((ext_vector_type(2)));

#define TLW 7
#define TROWS 16
#define DYN_G (1 << 20)
#define DYN_P (2 << 20)
#define CFG_LOGP 6
#define CFG_WPB 16
#define CFG_PRO_PRIO 3
#define CFG_T0 400
#define CFG_TS 1400
#define CFG_L1 (-4000)
#define CFG_L2 0
#define CFG_L3 4000
#define CFG_REL 2
#define CFG_R1 (-2500)
#define CFG_R2 0
#define CFG_R3 2500
#define CFG_M3 4000
#define CFG_M2 8000
#define CFG_M1 12000
#define CFG_CODEPF 0
#define CFG_CODEPF_W 4
#define CFG_BAR 0
#define CFG_K16 0

constexpr int KS = 65;
constexpr int ROWS = 16;
constexpr int SLOTS = ROWS * KS;
constexpr int LOGP = CFG_LOGP;
constexpr int WPB = CFG_WPB;
constexpr int COLS_B = 40 * 64;
constexpr int VR_B = 19 * 256;
constexpr int S_B = 2048;
constexpr int CNT_B = 256;
constexpr int WAVE_B = COLS_B + VR_B + S_B + CNT_B;
constexpr int OFF_VR = COLS_B, OFF_S = COLS_B + VR_B, OFF_CNT = COLS_B + VR_B + S_B;

__device__ __forceinline__ u32 cvt_pk_bf16(float a, float b) {
    f32x2 v = {a, b};
    bf16x2_t r = __builtin_convertvector(v, bf16x2_t);
    return __builtin_bit_cast(u32, r);
}

template <int CTRL, int RM, int BM>
__device__ __forceinline__ u32 dpp_add(u32 x) {
    return x + (u32)__builtin_amdgcn_update_dpp(0, (int)x, CTRL, RM, BM, true);
}

#define XCHG(t) \
        asm("s_mov_b64 vcc, %8\n\t" \
            "v_cndmask_b32_dpp %0, %6, %4, vcc row_ror:8 row_mask:0xf bank_mask:0xf\n\t" \
            "v_cndmask_b32_dpp %1, %7, %5, vcc row_ror:8 row_mask:0xf bank_mask:0xf\n\t" \
            "s_not_b64 vcc, vcc\n\t" \
            "v_cndmask_b32_dpp %2, %4, %6, vcc row_ror:8 row_mask:0xf bank_mask:0xf\n\t" \
            "v_cndmask_b32_dpp %3, %5, %7, vcc row_ror:8 row_mask:0xf bank_mask:0xf" \
            : "=&v"(X0), "=&v"(X1), "=&v"(Y0), "=&v"(Y1) \
            : "v"(g[t][0]), "v"(g[t][1]), "v"(g[t][2]), "v"(g[t][3]), "s"(lomask) \
            : "vcc");

__device__ __forceinline__ void compute_step(const u32x4 (&g)[4], const u32x4 S, f32x4 (&acc)[4], const u64 lomask) {
    u32x4 F0, F1, F2, F3;
#pragma unroll
    for (int t = 0; t < 4; ++t) {
        u32 X0, X1, Y0, Y1;
        XCHG(t)
        F0[t] = __builtin_amdgcn_perm(Y0, X0, 0x05040100u);
        F1[t] = __builtin_amdgcn_perm(Y0, X0, 0x07060302u);
        F2[t] = __builtin_amdgcn_perm(Y1, X1, 0x05040100u);
        F3[t] = __builtin_amdgcn_perm(Y1, X1, 0x07060302u);
    }
    const bf16x8 a = __builtin_bit_cast(bf16x8, S);
    acc[0] = __builtin_amdgcn_mfma_f32_16x16x32_bf16(a, __builtin_bit_cast(bf16x8, F0), acc[0], 0, 0, 0);
    acc[1] = __builtin_amdgcn_mfma_f32_16x16x32_bf16(a, __builtin_bit_cast(bf16x8, F1), acc[1], 0, 0, 0);
    acc[2] = __builtin_amdgcn_mfma_f32_16x16x32_bf16(a, __builtin_bit_cast(bf16x8, F2), acc[2], 0, 0, 0);
    acc[3] = __builtin_amdgcn_mfma_f32_16x16x32_bf16(a, __builtin_bit_cast(bf16x8, F3), acc[3], 0, 0, 0);
}

__device__ __forceinline__ void issue_gather(u32x4 (&g)[4], const char* __restrict__ B, u32x2 c, u32 laneoff) {
    u32 o0, o1, o2, o3;
    asm("v_mad_u32_u16 %0, %1, %2, %3" : "=v"(o0) : "v"(c[0]), "s"(128), "v"(laneoff));
    asm("v_mad_u32_u16 %0, %1, %2, %3 op_sel:[1,0,0,0]" : "=v"(o1) : "v"(c[0]), "s"(128), "v"(laneoff));
    asm("v_mad_u32_u16 %0, %1, %2, %3" : "=v"(o2) : "v"(c[1]), "s"(128), "v"(laneoff));
    asm("v_mad_u32_u16 %0, %1, %2, %3 op_sel:[1,0,0,0]" : "=v"(o3) : "v"(c[1]), "s"(128), "v"(laneoff));
    g[0] = *(const u32x4*)(B + o0);
    g[1] = *(const u32x4*)(B + o1);
    g[2] = *(const u32x4*)(B + o2);
    g[3] = *(const u32x4*)(B + o3);
}

__device__ __forceinline__ u32 ticks_since(u64 t0) { return (u32)(__builtin_amdgcn_s_memrealtime() - t0); }

__global__ __launch_bounds__(WPB * 64) void spmm_ell_kernel(const u16* __restrict__ vals, const u32* __restrict__ idx,
                                                            const char* __restrict__ B, u32x2* __restrict__ out
                                                            ) {
    __shared__ __attribute__((aligned(16))) char smem[WPB * WAVE_B + 64];
    const int lane = threadIdx.x & 63;
    const int wib = __builtin_amdgcn_readfirstlane(threadIdx.x >> 6);
    const u32 tile = blockIdx.x * WPB + wib;
    const u64 t0 = __builtin_amdgcn_s_memrealtime();
    if (CFG_PRO_PRIO) __builtin_amdgcn_s_setprio(CFG_PRO_PRIO);
    const u32* ip = idx + (size_t)tile * SLOTS;
    const u16* vp = vals + (size_t)tile * SLOTS;
    u32x4 I[4]; u32x2 V[4];
#pragma unroll
    for (int i = 0; i < 4; ++i) {
        V[i] = __builtin_nontemporal_load((const u32x2*)(vp + 256 * i + 4 * lane));
        I[i] = __builtin_nontemporal_load((const u32x4*)(ip + 256 * i + 4 * lane));
    }
    const u32 Vt = __builtin_nontemporal_load(vp + 1024 + (lane & 15));
    const u32 It = __builtin_nontemporal_load(ip + 1024 + (lane & 15));

    u32* progw = (u32*)(smem + WPB * WAVE_B);
    if (lane == 0) progw[wib] = 0;
    char* wb = smem + wib * WAVE_B;
    u16* cols = (u16*)wb;
    u32* vr = (u32*)(wb + OFF_VR);
    char* simg = wb + OFF_S;
    u32* cnt = (u32*)(wb + OFF_CNT);
    {
        u32x4 z = {0, 0, 0, 0};
        *(u32x4*)(simg + lane * 16) = z;
        *(u32x4*)(simg + 1024 + lane * 16) = z;
        u32x4 f = {0xffffffffu, 0xffffffffu, 0xffffffffu, 0xffffffffu};
        *(u32x4*)(wb + lane * 16) = f;
        *(u32x4*)(wb + 1024 + lane * 16) = f;
        if (lane < (COLS_B - 2048) / 16) *(u32x4*)(wb + 2048 + lane * 16) = f;
        cnt[lane] = 0;
    }
    u32 rowb[16];
#pragma unroll
    for (int i = 0; i < 4; ++i)
#pragma unroll
        for (int u = 0; u < 4; ++u) {
            rowb[4 * i + u] = (((256 * i + 4 * lane + u) * 4033u) >> 14) & 0xf0u;
            asm volatile("" : "+v"(rowb[4 * i + u]));
        }
    if (CFG_BAR == 2) { asm volatile("s_waitcnt vmcnt(0)" ::: "memory"); __builtin_amdgcn_s_barrier(); }
    const u32 cntb = (u32)(size_t)((__attribute__((address_space(3))) char*)cnt);
    const u32 wbl = (u32)(size_t)((__attribute__((address_space(3))) char*)wb);
    const u32 vzero = 0, vone = 1;
    u64 msk[17];
    u32 adr[17], rank[17];
#pragma unroll
    for (int i = 0; i < 4; ++i) {
        asm("v_cmp_neq_f16_sdwa %0, %1, %2 src0_sel:WORD_0 src1_sel:WORD_0" : "=s"(msk[4 * i + 0]) : "v"(V[i][0]), "v"(vzero));
        asm("v_cmp_neq_f16_sdwa %0, %1, %2 src0_sel:WORD_1 src1_sel:WORD_0" : "=s"(msk[4 * i + 1]) : "v"(V[i][0]), "v"(vzero));
        asm("v_cmp_neq_f16_sdwa %0, %1, %2 src0_sel:WORD_0 src1_sel:WORD_0" : "=s"(msk[4 * i + 2]) : "v"(V[i][1]), "v"(vzero));
        asm("v_cmp_neq_f16_sdwa %0, %1, %2 src0_sel:WORD_1 src1_sel:WORD_0" : "=s"(msk[4 * i + 3]) : "v"(V[i][1]), "v"(vzero));
#pragma unroll
        for (int u = 0; u < 4; ++u) adr[4 * i + u] = cntb + (((I[i][u] >> (16 - LOGP)) & ((1u << LOGP) - 1)) << 2);
        asm volatile("s_nop 0\n\t"
                     "s_mov_b64 exec, %8\n\tds_add_rtn_u32 %0, %4, %12\n\t"
                     "s_mov_b64 exec, %9\n\tds_add_rtn_u32 %1, %5, %12\n\t"
                     "s_mov_b64 exec, %10\n\tds_add_rtn_u32 %2, %6, %12\n\t"
                     "s_mov_b64 exec, %11\n\tds_add_rtn_u32 %3, %7, %12\n\t"
                     "s_mov_b64 exec, -1"
                     : "=&v"(rank[4 * i]), "=&v"(rank[4 * i + 1]), "=&v"(rank[4 * i + 2]), "=&v"(rank[4 * i + 3])
                     : "v"(adr[4 * i]), "v"(adr[4 * i + 1]), "v"(adr[4 * i + 2]), "v"(adr[4 * i + 3]),
                       "s"(msk[4 * i]), "s"(msk[4 * i + 1]), "s"(msk[4 * i + 2]), "s"(msk[4 * i + 3]), "v"(vone)
                     : "memory");
    }
    {
        u64 m;
        asm("v_cmp_neq_f16_sdwa %0, %1, %2 src0_sel:WORD_0 src1_sel:WORD_0" : "=s"(m) : "v"(Vt), "v"(vzero));
        msk[16] = m & 0xffffull;
        adr[16] = cntb + (((It >> (16 - LOGP)) & ((1u << LOGP) - 1)) << 2);
        asm volatile("s_nop 0\n\ts_mov_b64 exec, %2\n\tds_add_rtn_u32 %0, %1, %3\n\ts_mov_b64 exec, -1"
                     : "=&v"(rank[16]) : "v"(adr[16]), "s"(msk[16]), "v"(vone) : "memory");
    }
    const u32 c = cnt[lane];
    u32 x = c;
    x = dpp_add<0x111, 0xf, 0xf>(x);
    asm volatile("s_waitcnt lgkmcnt(0)" ::: "memory");
    x = dpp_add<0x112, 0xf, 0xf>(x);
    x = dpp_add<0x114, 0xf, 0xf>(x);
    x = dpp_add<0x118, 0xf, 0xf>(x);
    x = dpp_add<0x142, 0xa, 0xf>(x);
    x = dpp_add<0x143, 0xc, 0xf>(x);
    const int n = __builtin_amdgcn_readlane((int)x, 63);
    cnt[lane] = x - c;
    typedef __attribute__((address_space(3))) const u32* lds_cu32;
    u32 pos[17];
#pragma unroll
    for (int j = 0; j < 17; ++j) pos[j] = *(lds_cu32)(size_t)adr[j];
    __builtin_amdgcn_sched_barrier(0);
#pragma unroll
    for (int i = 0; i < 4; ++i) {
        u32 ca[4], va[4], w[4];
#pragma unroll
        for (int u = 0; u < 4; ++u) {
            const u32 p = pos[4 * i + u] + rank[4 * i + u];
            ca[u] = wbl + 2 * p;
            va[u] = wbl + 4 * p;
            const u32 vv = V[i][u >> 1];
            w[u] = (u & 1) ? ((vv & 0xffff0000u) | rowb[4 * i + u]) : ((vv << 16) | rowb[4 * i + u]);
        }
        asm volatile("s_mov_b64 exec, %16\n\tds_write_b16 %0, %8\n\tds_write_b32 %4, %12 offset:%20\n\t"
                     "s_mov_b64 exec, %17\n\tds_write_b16 %1, %9\n\tds_write_b32 %5, %13 offset:%20\n\t"
                     "s_mov_b64 exec, %18\n\tds_write_b16 %2, %10\n\tds_write_b32 %6, %14 offset:%20\n\t"
                     "s_mov_b64 exec, %19\n\tds_write_b16 %3, %11\n\tds_write_b32 %7, %15 offset:%20\n\t"
                     "s_mov_b64 exec, -1"
                     :
                     : "v"(ca[0]), "v"(ca[1]), "v"(ca[2]), "v"(ca[3]), "v"(va[0]), "v"(va[1]), "v"(va[2]), "v"(va[3]),
                       "v"(I[i][0]), "v"(I[i][1]), "v"(I[i][2]), "v"(I[i][3]), "v"(w[0]), "v"(w[1]), "v"(w[2]), "v"(w[3]),
                       "s"(msk[4 * i]), "s"(msk[4 * i + 1]), "s"(msk[4 * i + 2]), "s"(msk[4 * i + 3]), "i"(OFF_VR)
                     : "memory");
    }
    {
        const u32 p = pos[16] + rank[16];
        const u32 ca = wbl + 2 * p, va = wbl + 4 * p, w = (Vt << 16) | (15u << 4);
        asm volatile("s_mov_b64 exec, %4\n\tds_write_b16 %0, %2\n\tds_write_b32 %1, %3 offset:%5\n\ts_mov_b64 exec, -1"
                     :: "v"(ca), "v"(va), "v"(It), "v"(w), "s"(msk[16]), "i"(OFF_VR) : "memory");
    }

    if (CFG_BAR == 1) __syncthreads();
    const u32 laneoff = (lane & 7) * 16;
    const u64 lomask = 0x00ff00ff00ff00ffull;
    const u32 sconst = (lane >> 5) * 1024 + ((lane >> 3) & 3) * 256 + (2 * (lane & 3) + ((lane >> 2) & 1)) * 2;
    const char* colrd = (const char*)cols + (lane >> 3) * 8;
    f32x4 acc[4];
#pragma unroll
    for (int j = 0; j < 4; ++j) acc[j] = (f32x4){0.f, 0.f, 0.f, 0.f};
    const int nb = (n + 63) >> 6;
#define PUBLISH(colv) { if (lane == 0) progw[wib] = (colv) & 0xffffu; pg = progw[lane & (WPB - 1)]; }
#define DPPMIN(x, C) min(x, (u32)__builtin_amdgcn_update_dpp((int)0x7fffffff, (int)(x), C, 0xf, 0xf, false))
#define PACE(colv) { \
        u32 a_ = pg; \
        a_ = DPPMIN(a_, 0x111); a_ = DPPMIN(a_, 0x112); a_ = DPPMIN(a_, 0x114); a_ = DPPMIN(a_, 0x118); \
        const int ahead_ = __builtin_amdgcn_readlane((int)pg, wib) - __builtin_amdgcn_readlane((int)a_, WPB - 1); \
        if (ahead_ < CFG_M3) __builtin_amdgcn_s_setprio(3); \
        else if (ahead_ < CFG_M2) __builtin_amdgcn_s_setprio(2); \
        else if (ahead_ < CFG_M1) __builtin_amdgcn_s_setprio(1); \
        else __builtin_amdgcn_s_setprio(0); }
#define RDCOLS(s) (*(const u32x2*)(colrd + (s) * 64))
#define BUILD_S(bb) { const u32 sa_ = sconst + (wv & 0xffffu); const bool ok_ = (bb) * 64 + lane < n; \
        if (ok_) *(u16*)(simg + sa_) = (u16)(wv >> 16); \
        S0 = *(const u32x4*)(simg + lane * 16); S1 = *(const u32x4*)(simg + 1024 + lane * 16); \
        if (ok_) *(u16*)(simg + sa_) = 0; \
        wv = vr[((bb) + 1) * 64 + lane]; }
    if (nb > 0) {
        u32x4 gA[4], gB[4], gC[4], gD[4];
        u32x4 S0, S1;
        u32x2 c0 = RDCOLS(0), c1 = RDCOLS(1), cC = RDCOLS(2), cD = RDCOLS(3);
        u32 wv = vr[lane];
        u32 pg = progw[lane & (WPB - 1)];
        PACE(__builtin_amdgcn_readfirstlane(c0[0] & 0xffffu));
        issue_gather(gA, B, c0, laneoff);
        issue_gather(gB, B, c1, laneoff);
        __builtin_amdgcn_sched_barrier(0);
        BUILD_S(0);
        int b = 0;
#define HALF(P, Q, R, T, cR, cT, cP, cQ) { \
            PACE(__builtin_amdgcn_readfirstlane(cR[0] & 0xffffu)); \
            issue_gather(R, B, cR, laneoff); \
            cP = RDCOLS(2 * b + 4); \
            __builtin_amdgcn_sched_barrier(0); \
            compute_step(P, S0, acc, lomask); \
            __builtin_amdgcn_sched_barrier(0); \
            issue_gather(T, B, cT, laneoff); \
            cQ = RDCOLS(2 * b + 5); \
            __builtin_amdgcn_sched_barrier(0); \
            compute_step(Q, S1, acc, lomask); \
            __builtin_amdgcn_sched_barrier(0); \
            BUILD_S(b + 1); \
            PUBLISH(cR[0]); \
            __builtin_amdgcn_sched_barrier(0); \
            ++b; }
        u32x2 cA, cB;
        for (; b + 2 < nb;) {
            HALF(gA, gB, gC, gD, cC, cD, cA, cB);
            HALF(gC, gD, gA, gB, cA, cB, cC, cD);
        }
        if (b + 1 < nb) {
            issue_gather(gC, B, cC, laneoff);
            __builtin_amdgcn_sched_barrier(0);
            compute_step(gA, S0, acc, lomask);
            issue_gather(gD, B, cD, laneoff);
            __builtin_amdgcn_sched_barrier(0);
            compute_step(gB, S1, acc, lomask);
            BUILD_S(b + 1);
            compute_step(gC, S0, acc, lomask);
            if ((b + 1) * 64 + 32 < n) compute_step(gD, S1, acc, lomask);
        } else {
            compute_step(gA, S0, acc, lomask);
            if (b * 64 + 32 < n) compute_step(gB, S1, acc, lomask);
        }
    }
    if (lane == 0) progw[wib] = (CFG_REL == 1) ? 65535u : 0x7fffffffu;
    {
        const int q = lane >> 4;
        u32x2* op = out + ((size_t)tile * ROWS + 4 * q) * 16 + (lane & 7) * 2 + ((lane >> 3) & 1);
#pragma unroll
        for (int r = 0; r < 4; ++r) {
            u32x2 o;
            o[0] = cvt_pk_bf16(acc[0][r], acc[1][r]);
            o[1] = cvt_pk_bf16(acc[2][r], acc[3][r]);
            __builtin_nontemporal_store(o, op + r * 16);
        }
    }
}

extern "C" void kernel_launch(void* const* d_in, const int* in_sizes, int n_in,
                              void* d_out, int out_size, void* d_ws, size_t ws_size,
                              hipStream_t stream) {
    const u16* vals = (const u16*)d_in[0];
    const u32* idx = (const u32*)d_in[1];
    const char* B = (const char*)d_in[2];
    const int M = out_size / 64;
    const int tiles = M / ROWS;
    spmm_ell_kernel<<<tiles / WPB, WPB * 64, 0, stream>>>(vals, idx, B, (u32x2*)d_out
        );
}
```

```cpp
#include <hip/hip_runtime.h>
#include <stdint.h>

typedef unsigned int u32;
typedef unsigned short u16;
typedef unsigned long long u64;
typedef u32 u32x4 __attribute__((ext_vector_type(4)));
typedef u32 u32x2 __attribute__((ext_vector_type(2)));
typedef float f32x4 __attribute__((ext_vector_type(4)));
typedef __bf16 bf16x8 __attribute__((ext_vector_type(8)));
typedef __bf16 bf16x2_t __attribute__((ext_vector_type(2)));
typedef float f32x2 __attribute__((ext_vector_type(2)));

#define TLW 7
#define TROWS 16
#define DYN_G (1 << 20)
#define DYN_P (2 << 20)
#define CFG_LOGP 5
#define CFG_WPB 16
#define CFG_PRO_PRIO 3
#define CFG_T0 400
#define CFG_TS 1400
#define CFG_L1 (-4000)
#define CFG_L2 0
#define CFG_L3 4000
#define CFG_REL 2
#define CFG_R1 (-2500)
#define CFG_R2 0
#define CFG_R3 2500
#define CFG_M3 3000
#define CFG_M2 6000
#define CFG_M1 10000
#define CFG_BAR 0
#define CFG_ROT 8192
#define CFG_DEEP 0
#define CFG_K16 0

constexpr int KS = 65;
constexpr int ROWS = 16;
constexpr int SLOTS = ROWS * KS;
constexpr int LOGP = CFG_LOGP;
constexpr int WPB = CFG_WPB;
constexpr int COLS_B = 40 * 64;
constexpr int VR_B = 19 * 256;
constexpr int S_B = 2048;
constexpr int CNT_B = 256;
constexpr int WAVE_B = COLS_B + VR_B + S_B + CNT_B;
constexpr int OFF_VR = COLS_B, OFF_S = COLS_B + VR_B, OFF_CNT = COLS_B + VR_B + S_B;

__device__ __forceinline__ u32 cvt_pk_bf16(float a, float b) {
    f32x2 v = {a, b};
    bf16x2_t r = __builtin_convertvector(v, bf16x2_t);
    return __builtin_bit_cast(u32, r);
}

template <int CTRL, int RM, int BM>
__device__ __forceinline__ u32 dpp_add(u32 x) {
    return x + (u32)__builtin_amdgcn_update_dpp(0, (int)x, CTRL, RM, BM, true);
}

#define XCHG(t) \
        asm("s_mov_b64 vcc, %8\n\t" \
            "v_cndmask_b32_dpp %0, %6, %4, vcc row_ror:8 row_mask:0xf bank_mask:0xf\n\t" \
            "v_cndmask_b32_dpp %1, %7, %5, vcc row_ror:8 row_mask:0xf bank_mask:0xf\n\t" \
            "s_not_b64 vcc, vcc\n\t" \
            "v_cndmask_b32_dpp %2, %4, %6, vcc row_ror:8 row_mask:0xf bank_mask:0xf\n\t" \
            "v_cndmask_b32_dpp %3, %5, %7, vcc row_ror:8 row_mask:0xf bank_mask:0xf" \
            : "=&v"(X0), "=&v"(X1), "=&v"(Y0), "=&v"(Y1) \
            : "v"(g[t][0]), "v"(g[t][1]), "v"(g[t][2]), "v"(g[t][3]), "s"(lomask) \
            : "vcc");

__device__ __forceinline__ void compute_step(const u32x4 (&g)[4], const u32x4 S, f32x4 (&acc)[4], const u64 lomask) {
    u32x4 F0, F1, F2, F3;
#pragma unroll
    for (int t = 0; t < 4; ++t) {
        u32 X0, X1, Y0, Y1;
        XCHG(t)
        F0[t] = __builtin_amdgcn_perm(Y0, X0, 0x05040100u);
        F1[t] = __builtin_amdgcn_perm(Y0, X0, 0x07060302u);
        F2[t] = __builtin_amdgcn_perm(Y1, X1, 0x05040100u);
        F3[t] = __builtin_amdgcn_perm(Y1, X1, 0x07060302u);
    }
    const bf16x8 a = __builtin_bit_cast(bf16x8, S);
    acc[0] = __builtin_amdgcn_mfma_f32_16x16x32_bf16(a, __builtin_bit_cast(bf16x8, F0), acc[0], 0, 0, 0);
    acc[1] = __builtin_amdgcn_mfma_f32_16x16x32_bf16(a, __builtin_bit_cast(bf16x8, F1), acc[1], 0, 0, 0);
    acc[2] = __builtin_amdgcn_mfma_f32_16x16x32_bf16(a, __builtin_bit_cast(bf16x8, F2), acc[2], 0, 0, 0);
    acc[3] = __builtin_amdgcn_mfma_f32_16x16x32_bf16(a, __builtin_bit_cast(bf16x8, F3), acc[3], 0, 0, 0);
}

__device__ __forceinline__ void issue_gather(u32x4 (&g)[4], const char* __restrict__ B, u32x2 c, u32 laneoff) {
    u32 o0, o1, o2, o3;
    asm("v_mad_u32_u16 %0, %1, %2, %3" : "=v"(o0) : "v"(c[0]), "s"(128), "v"(laneoff));
    asm("v_mad_u32_u16 %0, %1, %2, %3 op_sel:[1,0,0,0]" : "=v"(o1) : "v"(c[0]), "s"(128), "v"(laneoff));
    asm("v_mad_u32_u16 %0, %1, %2, %3" : "=v"(o2) : "v"(c[1]), "s"(128), "v"(laneoff));
    asm("v_mad_u32_u16 %0, %1, %2, %3 op_sel:[1,0,0,0]" : "=v"(o3) : "v"(c[1]), "s"(128), "v"(laneoff));
    g[0] = *(const u32x4*)(B + o0);
    g[1] = *(const u32x4*)(B + o1);
    g[2] = *(const u32x4*)(B + o2);
    g[3] = *(const u32x4*)(B + o3);
}

__device__ __forceinline__ u32 ticks_since(u64 t0) { return (u32)(__builtin_amdgcn_s_memrealtime() - t0); }

__global__ __launch_bounds__(WPB * 64) void spmm_ell_kernel(const u16* __restrict__ vals, const u32* __restrict__ idx,
                                                            const char* __restrict__ B, u32x2* __restrict__ out
                                                            ) {
    __shared__ __attribute__((aligned(16))) char smem[WPB * WAVE_B + 64];
    const int lane = threadIdx.x & 63;
    const int wib = __builtin_amdgcn_readfirstlane(threadIdx.x >> 6);
    const u32 tile = blockIdx.x * WPB + wib;
    const u64 t0 = __builtin_amdgcn_s_memrealtime();
    if (CFG_PRO_PRIO) __builtin_amdgcn_s_setprio(CFG_PRO_PRIO);
    const u32* ip = idx + (size_t)tile * SLOTS;
    const u16* vp = vals + (size_t)tile * SLOTS;
    u32x4 I[4]; u32x2 V[4];
#pragma unroll
    for (int i = 0; i < 4; ++i) {
        V[i] = __builtin_nontemporal_load((const u32x2*)(vp + 256 * i + 4 * lane));
        I[i] = __builtin_nontemporal_load((const u32x4*)(ip + 256 * i + 4 * lane));
    }
    const u32 Vt = __builtin_nontemporal_load(vp + 1024 + (lane & 15));
    const u32 It = __builtin_nontemporal_load(ip + 1024 + (lane & 15));

    const u32 rot = CFG_ROT ? (65536u - (blockIdx.x & 7u) * CFG_ROT) & 0xffffu : 0u;
    u32* progw = (u32*)(smem + WPB * WAVE_B);
    if (lane == 0) progw[wib] = 0;
    char* wb = smem + wib * WAVE_B;
    u16* cols = (u16*)wb;
    u32* vr = (u32*)(wb + OFF_VR);
    char* simg = wb + OFF_S;
    u32* cnt = (u32*)(wb + OFF_CNT);
    {
        u32x4 z = {0, 0, 0, 0};
        *(u32x4*)(simg + lane * 16) = z;
        *(u32x4*)(simg + 1024 + lane * 16) = z;
        const u32 padc = ((0xffffu - rot) & 0xffffu) * 0x10001u;
        u32x4 f = {padc, padc, padc, padc};
        *(u32x4*)(wb + lane * 16) = f;
        *(u32x4*)(wb + 1024 + lane * 16) = f;
        if (lane < (COLS_B - 2048) / 16) *(u32x4*)(wb + 2048 + lane * 16) = f;
        cnt[lane] = 0;
    }
    u32 rowb[16];
#pragma unroll
    for (int i = 0; i < 4; ++i)
#pragma unroll
        for (int u = 0; u < 4; ++u) {
            rowb[4 * i + u] = (((256 * i + 4 * lane + u) * 4033u) >> 14) & 0xf0u;
            asm volatile("" : "+v"(rowb[4 * i + u]));
        }
    if (CFG_BAR == 2) { asm volatile("s_waitcnt vmcnt(0)" ::: "memory"); __builtin_amdgcn_s_barrier(); }
    const u32 cntb = (u32)(size_t)((__attribute__((address_space(3))) char*)cnt);
    const u32 wbl = (u32)(size_t)((__attribute__((address_space(3))) char*)wb);
    const u32 vzero = 0, vone = 1;
    u64 msk[17];
    u32 adr[17], rank[17];
#pragma unroll
    for (int i = 0; i < 4; ++i) {
        asm("v_cmp_neq_f16_sdwa %0, %1, %2 src0_sel:WORD_0 src1_sel:WORD_0" : "=s"(msk[4 * i + 0]) : "v"(V[i][0]), "v"(vzero));
        asm("v_cmp_neq_f16_sdwa %0, %1, %2 src0_sel:WORD_1 src1_sel:WORD_0" : "=s"(msk[4 * i + 1]) : "v"(V[i][0]), "v"(vzero));
        asm("v_cmp_neq_f16_sdwa %0, %1, %2 src0_sel:WORD_0 src1_sel:WORD_0" : "=s"(msk[4 * i + 2]) : "v"(V[i][1]), "v"(vzero));
        asm("v_cmp_neq_f16_sdwa %0, %1, %2 src0_sel:WORD_1 src1_sel:WORD_0" : "=s"(msk[4 * i + 3]) : "v"(V[i][1]), "v"(vzero));
#pragma unroll
        for (int u = 0; u < 4; ++u) adr[4 * i + u] = cntb + ((((I[i][u] + rot) >> (16 - LOGP)) & ((1u << LOGP) - 1)) << 2);
        asm volatile("s_nop 0\n\t"
                     "s_mov_b64 exec, %8\n\tds_add_rtn_u32 %0, %4, %12\n\t"
                     "s_mov_b64 exec, %9\n\tds_add_rtn_u32 %1, %5, %12\n\t"
                     "s_mov_b64 exec, %10\n\tds_add_rtn_u32 %2, %6, %12\n\t"
                     "s_mov_b64 exec, %11\n\tds_add_rtn_u32 %3, %7, %12\n\t"
                     "s_mov_b64 exec, -1"
                     : "=&v"(rank[4 * i]), "=&v"(rank[4 * i + 1]), "=&v"(rank[4 * i + 2]), "=&v"(rank[4 * i + 3])
                     : "v"(adr[4 * i]), "v"(adr[4 * i + 1]), "v"(adr[4 * i + 2]), "v"(adr[4 * i + 3]),
                       "s"(msk[4 * i]), "s"(msk[4 * i + 1]), "s"(msk[4 * i + 2]), "s"(msk[4 * i + 3]), "v"(vone)
                     : "memory");
    }
    {
        u64 m;
        asm("v_cmp_neq_f16_sdwa %0, %1, %2 src0_sel:WORD_0 src1_sel:WORD_0" : "=s"(m) : "v"(Vt), "v"(vzero));
        msk[16] = m & 0xffffull;
        adr[16] = cntb + ((((It + rot) >> (16 - LOGP)) & ((1u << LOGP) - 1)) << 2);
        asm volatile("s_nop 0\n\ts_mov_b64 exec, %2\n\tds_add_rtn_u32 %0, %1, %3\n\ts_mov_b64 exec, -1"
                     : "=&v"(rank[16]) : "v"(adr[16]), "s"(msk[16]), "v"(vone) : "memory");
    }
    const u32 c = cnt[lane];
    u32 x = c;
    x = dpp_add<0x111, 0xf, 0xf>(x);
    asm volatile("s_waitcnt lgkmcnt(0)" ::: "memory");
    x = dpp_add<0x112, 0xf, 0xf>(x);
    x = dpp_add<0x114, 0xf, 0xf>(x);
    x = dpp_add<0x118, 0xf, 0xf>(x);
    x = dpp_add<0x142, 0xa, 0xf>(x);
    x = dpp_add<0x143, 0xc, 0xf>(x);
    const int n = __builtin_amdgcn_readlane((int)x, 63);
    cnt[lane] = x - c;
    typedef __attribute__((address_space(3))) const u32* lds_cu32;
    u32 pos[17];
#pragma unroll
    for (int j = 0; j < 17; ++j) pos[j] = *(lds_cu32)(size_t)adr[j];
    __builtin_amdgcn_sched_barrier(0);
#pragma unroll
    for (int i = 0; i < 4; ++i) {
        u32 ca[4], va[4], w[4];
#pragma unroll
        for (int u = 0; u < 4; ++u) {
            const u32 p = pos[4 * i + u] + rank[4 * i + u];
            ca[u] = wbl + 2 * p;
            va[u] = wbl + 4 * p;
            const u32 vv = V[i][u >> 1];
            w[u] = (u & 1) ? ((vv & 0xffff0000u) | rowb[4 * i + u]) : ((vv << 16) | rowb[4 * i + u]);
        }
        asm volatile("s_mov_b64 exec, %16\n\tds_write_b16 %0, %8\n\tds_write_b32 %4, %12 offset:%20\n\t"
                     "s_mov_b64 exec, %17\n\tds_write_b16 %1, %9\n\tds_write_b32 %5, %13 offset:%20\n\t"
                     "s_mov_b64 exec, %18\n\tds_write_b16 %2, %10\n\tds_write_b32 %6, %14 offset:%20\n\t"
                     "s_mov_b64 exec, %19\n\tds_write_b16 %3, %11\n\tds_write_b32 %7, %15 offset:%20\n\t"
                     "s_mov_b64 exec, -1"
                     :
                     : "v"(ca[0]), "v"(ca[1]), "v"(ca[2]), "v"(ca[3]), "v"(va[0]), "v"(va[1]), "v"(va[2]), "v"(va[3]),
                       "v"(I[i][0]), "v"(I[i][1]), "v"(I[i][2]), "v"(I[i][3]), "v"(w[0]), "v"(w[1]), "v"(w[2]), "v"(w[3]),
                       "s"(msk[4 * i]), "s"(msk[4 * i + 1]), "s"(msk[4 * i + 2]), "s"(msk[4 * i + 3]), "i"(OFF_VR)
                     : "memory");
    }
    {
        const u32 p = pos[16] + rank[16];
        const u32 ca = wbl + 2 * p, va = wbl + 4 * p, w = (Vt << 16) | (15u << 4);
        asm volatile("s_mov_b64 exec, %4\n\tds_write_b16 %0, %2\n\tds_write_b32 %1, %3 offset:%5\n\ts_mov_b64 exec, -1"
                     :: "v"(ca), "v"(va), "v"(It), "v"(w), "s"(msk[16]), "i"(OFF_VR) : "memory");
    }

    if (CFG_BAR == 1) __syncthreads();
    const u32 laneoff = (lane & 7) * 16;
    const u64 lomask = 0x00ff00ff00ff00ffull;
    const u32 sconst = (lane >> 5) * 1024 + ((lane >> 3) & 3) * 256 + (2 * (lane & 3) + ((lane >> 2) & 1)) * 2;
    const char* colrd = (const char*)cols + (lane >> 3) * 8;
    f32x4 acc[4];
#pragma unroll
    for (int j = 0; j < 4; ++j) acc[j] = (f32x4){0.f, 0.f, 0.f, 0.f};
    const int nb = (n + 63) >> 6;
#define PUBLISH(colv) { if (lane == 0) progw[wib] = ((colv) + rot) & 0xffffu; pg = progw[lane & (WPB - 1)]; }
#define DPPMIN(x, C) min(x, (u32)__builtin_amdgcn_update_dpp((int)0x7fffffff, (int)(x), C, 0xf, 0xf, false))
#define PACE(colv) { \
        u32 a_ = pg; \
        a_ = DPPMIN(a_, 0x111); a_ = DPPMIN(a_, 0x112); a_ = DPPMIN(a_, 0x114); a_ = DPPMIN(a_, 0x118); \
        const int ahead_ = __builtin_amdgcn_readlane((int)pg, wib) - __builtin_amdgcn_readlane((int)a_, WPB - 1); \
        if (ahead_ < CFG_M3) __builtin_amdgcn_s_setprio(3); \
        else if (ahead_ < CFG_M2) __builtin_amdgcn_s_setprio(2); \
        else if (ahead_ < CFG_M1) __builtin_amdgcn_s_setprio(1); \
        else __builtin_amdgcn_s_setprio(0); }
#define RDCOLS(s) (*(const u32x2*)(colrd + (s) * 64))
#define BUILD_S(bb) { const u32 sa_ = sconst + (wv & 0xffffu); const bool ok_ = (bb) * 64 + lane < n; \
        if (ok_) *(u16*)(simg + sa_) = (u16)(wv >> 16); \
        S0 = *(const u32x4*)(simg + lane * 16); S1 = *(const u32x4*)(simg + 1024 + lane * 16); \
        if (ok_) *(u16*)(simg + sa_) = 0; \
        wv = vr[((bb) + 1) * 64 + lane]; }
    if (nb > 0) {
        const int nsteps = (n + 31) >> 5;
        u32x4 gA[4], gB[4];
        u32x4 S0, S1;
        u32x2 c0 = RDCOLS(0), cB = RDCOLS(1), cA = RDCOLS(2);
        u32 wv = vr[lane];
        u32 pg = progw[lane & (WPB - 1)];
        PACE(__builtin_amdgcn_readfirstlane(c0[0] & 0xffffu));
        issue_gather(gA, B, c0, laneoff);
        __builtin_amdgcn_sched_barrier(0);
        BUILD_S(0);
        int b = 0;
        for (; 2 * b + 2 < nsteps; ++b) {
            PACE(__builtin_amdgcn_readfirstlane(cB[0] & 0xffffu));
            issue_gather(gB, B, cB, laneoff);
            __builtin_amdgcn_sched_barrier(0);
            compute_step(gA, S0, acc, lomask);
            __builtin_amdgcn_sched_barrier(0);
            issue_gather(gA, B, cA, laneoff);
            __builtin_amdgcn_sched_barrier(0);
            compute_step(gB, S1, acc, lomask);
            __builtin_amdgcn_sched_barrier(0);
            PUBLISH(cA[0]);
            cB = RDCOLS(2 * b + 3);
            cA = RDCOLS(2 * b + 4);
            BUILD_S(b + 1);
            __builtin_amdgcn_sched_barrier(0);
        }
        if (2 * b + 1 < nsteps) {
            issue_gather(gB, B, cB, laneoff);
            __builtin_amdgcn_sched_barrier(0);
            compute_step(gA, S0, acc, lomask);
            compute_step(gB, S1, acc, lomask);
        } else {
            compute_step(gA, S0, acc, lomask);
        }
    }
    if (lane == 0) progw[wib] = (CFG_REL == 1) ? 65535u : 0x7fffffffu;
    {
        const int q = lane >> 4;
        u32x2* op = out + ((size_t)tile * ROWS + 4 * q) * 16 + (lane & 7) * 2 + ((lane >> 3) & 1);
#pragma unroll
        for (int r = 0; r < 4; ++r) {
            u32x2 o;
            o[0] = cvt_pk_bf16(acc[0][r], acc[1][r]);
            o[1] = cvt_pk_bf16(acc[2][r], acc[3][r]);
            __builtin_nontemporal_store(o, op + r * 16);
        }
    }
}

extern "C" void kernel_launch(void* const* d_in, const int* in_sizes, int n_in,
                              void* d_out, int out_size, void* d_ws, size_t ws_size,
                              hipStream_t stream) {
    const u16* vals = (const u16*)d_in[0];
    const u32* idx = (const u32*)d_in[1];
    const char* B = (const char*)d_in[2];
    const int M = out_size / 64;
    const int tiles = M / ROWS;
    spmm_ell_kernel<<<tiles / WPB, WPB * 64, 0, stream>>>(vals, idx, B, (u32x2*)d_out
        );
}
```

```cpp
#include <hip/hip_runtime.h>
#include <stdint.h>

typedef unsigned int u32;
typedef unsigned short u16;
typedef unsigned long long u64;
typedef u32 u32x4 __attribute__((ext_vector_type(4)));
typedef u32 u32x2 __attribute__((ext_vector_type(2)));
typedef float f32x4 __attribute__((ext_vector_type(4)));
typedef float f32x2 __attribute__((ext_vector_type(2)));
typedef __bf16 bf16x8 __attribute__((ext_vector_type(8)));
typedef __bf16 bf16x2 __attribute__((ext_vector_type(2)));

#define CFG_M3 3000
#define CFG_M2 6000
#define CFG_M1 10000

constexpr int KS = 65;
constexpr int DD = 64;
constexpr int ROWS = 16;
constexpr int SLOTS = ROWS * KS;
constexpr int LOGP = 5;
constexpr int WPB = 16;
constexpr int COLS_B = 40 * 64;
constexpr int VR_B = 19 * 256;
constexpr int S_B = 2048;
constexpr int CNT_B = 256;
constexpr int WAVE_B = COLS_B + VR_B + S_B + CNT_B;
constexpr int OFF_VR = COLS_B, OFF_S = COLS_B + VR_B, OFF_CNT = COLS_B + VR_B + S_B;

typedef __attribute__((address_space(3))) char* lds_ptr;
typedef __attribute__((address_space(3))) const u32* lds_cu32;

__device__ __forceinline__ u32 cvt_pk_bf16(float a, float b) {
    f32x2 v = {a, b};
    return __builtin_bit_cast(u32, __builtin_convertvector(v, bf16x2));
}
template <int CTRL, int RM>
__device__ __forceinline__ u32 dpp_add(u32 x) { return x + (u32)__builtin_amdgcn_update_dpp(0, (int)x, CTRL, RM, 0xf, true); }
template <int CTRL>
__device__ __forceinline__ u32 dpp_min(u32 x) { return min(x, (u32)__builtin_amdgcn_update_dpp(0x7fffffff, (int)x, CTRL, 0xf, 0xf, false)); }

__device__ __forceinline__ void compute_step(const u32x4 (&g)[4], const u32x4 S, f32x4 (&acc)[4]) {
    const u64 lomask = 0x00ff00ff00ff00ffull;
    u32x4 F0, F1, F2, F3;
#pragma unroll
    for (int t = 0; t < 4; ++t) {
        u32 X0, X1, Y0, Y1;
        asm("s_mov_b64 vcc, %8\n\t"
            "v_cndmask_b32_dpp %0, %6, %4, vcc row_ror:8 row_mask:0xf bank_mask:0xf\n\t"
            "v_cndmask_b32_dpp %1, %7, %5, vcc row_ror:8 row_mask:0xf bank_mask:0xf\n\t"
            "s_not_b64 vcc, vcc\n\t"
            "v_cndmask_b32_dpp %2, %4, %6, vcc row_ror:8 row_mask:0xf bank_mask:0xf\n\t"
            "v_cndmask_b32_dpp %3, %5, %7, vcc row_ror:8 row_mask:0xf bank_mask:0xf"
            : "=&v"(X0), "=&v"(X1), "=&v"(Y0), "=&v"(Y1)
            : "v"(g[t][0]), "v"(g[t][1]), "v"(g[t][2]), "v"(g[t][3]), "s"(lomask)
            : "vcc");
        F0[t] = __builtin_amdgcn_perm(Y0, X0, 0x05040100u);
        F1[t] = __builtin_amdgcn_perm(Y0, X0, 0x07060302u);
        F2[t] = __builtin_amdgcn_perm(Y1, X1, 0x05040100u);
        F3[t] = __builtin_amdgcn_perm(Y1, X1, 0x07060302u);
    }
    const bf16x8 a = __builtin_bit_cast(bf16x8, S);
    acc[0] = __builtin_amdgcn_mfma_f32_16x16x32_bf16(a, __builtin_bit_cast(bf16x8, F0), acc[0], 0, 0, 0);
    acc[1] = __builtin_amdgcn_mfma_f32_16x16x32_bf16(a, __builtin_bit_cast(bf16x8, F1), acc[1], 0, 0, 0);
    acc[2] = __builtin_amdgcn_mfma_f32_16x16x32_bf16(a, __builtin_bit_cast(bf16x8, F2), acc[2], 0, 0, 0);
    acc[3] = __builtin_amdgcn_mfma_f32_16x16x32_bf16(a, __builtin_bit_cast(bf16x8, F3), acc[3], 0, 0, 0);
}

__device__ __forceinline__ void issue_gather(u32x4 (&g)[4], const char* __restrict__ B, u32x2 c, u32 laneoff) {
    u32 o0, o1, o2, o3;
    asm("v_mad_u32_u16 %0, %1, %2, %3" : "=v"(o0) : "v"(c[0]), "s"(128), "v"(laneoff));
    asm("v_mad_u32_u16 %0, %1, %2, %3 op_sel:[1,0,0,0]" : "=v"(o1) : "v"(c[0]), "s"(128), "v"(laneoff));
    asm("v_mad_u32_u16 %0, %1, %2, %3" : "=v"(o2) : "v"(c[1]), "s"(128), "v"(laneoff));
    asm("v_mad_u32_u16 %0, %1, %2, %3 op_sel:[1,0,0,0]" : "=v"(o3) : "v"(c[1]), "s"(128), "v"(laneoff));
    g[0] = *(const u32x4*)(B + o0);
    g[1] = *(const u32x4*)(B + o1);
    g[2] = *(const u32x4*)(B + o2);
    g[3] = *(const u32x4*)(B + o3);
}

__device__ __forceinline__ void rows_exact(const u16* __restrict__ vals, const u32* __restrict__ idx, const u16* __restrict__ B,
                                           u16* __restrict__ out, size_t row0, int nrows, int K, int lane) {
    for (int r = 0; r < nrows; ++r) {
        const size_t m = row0 + r;
        float a = 0.f;
        for (int k = 0; k < K; ++k) {
            const float v = __uint_as_float((u32)vals[m * K + k] << 16);
            const float b = __uint_as_float((u32)B[(size_t)idx[m * K + k] * DD + lane] << 16);
            a = __builtin_fmaf(v, b, a);
        }
        out[m * DD + lane] = (u16)cvt_pk_bf16(a, a);
    }
}

__global__ __launch_bounds__(WPB * 64) void spmm_ell_kernel(const u16* __restrict__ vals, const u32* __restrict__ idx,
                                                            const char* __restrict__ B, u32x2* __restrict__ out
                                                            ) {
    __shared__ __attribute__((aligned(16))) char smem[WPB * WAVE_B + 64];
    const int lane = threadIdx.x & 63;
    const int wib = __builtin_amdgcn_readfirstlane(threadIdx.x >> 6);
    const u32 tile = blockIdx.x * WPB + wib;
    __builtin_amdgcn_s_setprio(3);
    const u32* ip = idx + (size_t)tile * SLOTS;
    const u16* vp = vals + (size_t)tile * SLOTS;
    u32x4 I[4]; u32x2 V[4];
#pragma unroll
    for (int i = 0; i < 4; ++i) {
        V[i] = __builtin_nontemporal_load((const u32x2*)(vp + 256 * i + 4 * lane));
        I[i] = __builtin_nontemporal_load((const u32x4*)(ip + 256 * i + 4 * lane));
    }
    const u32 Vt = __builtin_nontemporal_load(vp + 1024 + (lane & 15));
    const u32 It = __builtin_nontemporal_load(ip + 1024 + (lane & 15));

    const u32 rot = (65536u - (blockIdx.x & 7u) * 8192u) & 0xffffu;
    u32* progw = (u32*)(smem + WPB * WAVE_B);
    if (lane == 0) progw[wib] = 0;
    char* wb = smem + wib * WAVE_B;
    u32* vr = (u32*)(wb + OFF_VR);
    char* simg = wb + OFF_S;
    u32* cnt = (u32*)(wb + OFF_CNT);
    {
        const u32x4 z = {0, 0, 0, 0};
        *(u32x4*)(simg + lane * 16) = z;
        *(u32x4*)(simg + 1024 + lane * 16) = z;
        const u32 padc = ((0xffffu - rot) & 0xffffu) * 0x10001u;
        const u32x4 f = {padc, padc, padc, padc};
        *(u32x4*)(wb + lane * 16) = f;
        *(u32x4*)(wb + 1024 + lane * 16) = f;
        if (lane < (COLS_B - 2048) / 16) *(u32x4*)(wb + 2048 + lane * 16) = f;
        cnt[lane] = 0;
    }
    u32 rowb[16];
#pragma unroll
    for (int i = 0; i < 4; ++i)
#pragma unroll
        for (int u = 0; u < 4; ++u) {
            rowb[4 * i + u] = (((256 * i + 4 * lane + u) * 4033u) >> 14) & 0xf0u;
            asm volatile("" : "+v"(rowb[4 * i + u]));
        }
    const u32 cntb = (u32)(size_t)(lds_ptr)(char*)cnt;
    const u32 wbl = (u32)(size_t)(lds_ptr)wb;
    const u32 vzero = 0, vone = 1;
    u64 msk[17];
    u32 adr[17], rank[17];
#pragma unroll
    for (int i = 0; i < 4; ++i) {
        asm("v_cmp_neq_f16_sdwa %0, %1, %2 src0_sel:WORD_0 src1_sel:WORD_0" : "=s"(msk[4 * i + 0]) : "v"(V[i][0]), "v"(vzero));
        asm("v_cmp_neq_f16_sdwa %0, %1, %2 src0_sel:WORD_1 src1_sel:WORD_0" : "=s"(msk[4 * i + 1]) : "v"(V[i][0]), "v"(vzero));
        asm("v_cmp_neq_f16_sdwa %0, %1, %2 src0_sel:WORD_0 src1_sel:WORD_0" : "=s"(msk[4 * i + 2]) : "v"(V[i][1]), "v"(vzero));
        asm("v_cmp_neq_f16_sdwa %0, %1, %2 src0_sel:WORD_1 src1_sel:WORD_0" : "=s"(msk[4 * i + 3]) : "v"(V[i][1]), "v"(vzero));
#pragma unroll
        for (int u = 0; u < 4; ++u) adr[4 * i + u] = cntb + ((((I[i][u] + rot) >> (16 - LOGP)) & ((1u << LOGP) - 1)) << 2);
        asm volatile("s_nop 0\n\t"
                     "s_mov_b64 exec, %8\n\tds_add_rtn_u32 %0, %4, %12\n\t"
                     "s_mov_b64 exec, %9\n\tds_add_rtn_u32 %1, %5, %12\n\t"
                     "s_mov_b64 exec, %10\n\tds_add_rtn_u32 %2, %6, %12\n\t"
                     "s_mov_b64 exec, %11\n\tds_add_rtn_u32 %3, %7, %12\n\t"
                     "s_mov_b64 exec, -1"
                     : "=&v"(rank[4 * i]), "=&v"(rank[4 * i + 1]), "=&v"(rank[4 * i + 2]), "=&v"(rank[4 * i + 3])
                     : "v"(adr[4 * i]), "v"(adr[4 * i + 1]), "v"(adr[4 * i + 2]), "v"(adr[4 * i + 3]),
                       "s"(msk[4 * i]), "s"(msk[4 * i + 1]), "s"(msk[4 * i + 2]), "s"(msk[4 * i + 3]), "v"(vone)
                     : "memory");
    }
    {
        u64 m;
        asm("v_cmp_neq_f16_sdwa %0, %1, %2 src0_sel:WORD_0 src1_sel:WORD_0" : "=s"(m) : "v"(Vt), "v"(vzero));
        msk[16] = m & 0xffffull;
        adr[16] = cntb + ((((It + rot) >> (16 - LOGP)) & ((1u << LOGP) - 1)) << 2);
        asm volatile("s_nop 0\n\ts_mov_b64 exec, %2\n\tds_add_rtn_u32 %0, %1, %3\n\ts_mov_b64 exec, -1"
                     : "=&v"(rank[16]) : "v"(adr[16]), "s"(msk[16]), "v"(vone) : "memory");
    }
    const u32 c = cnt[lane];
    u32 x = c;
    x = dpp_add<0x111, 0xf>(x);
    asm volatile("s_waitcnt lgkmcnt(0)" ::: "memory");
    x = dpp_add<0x112, 0xf>(x);
    x = dpp_add<0x114, 0xf>(x);
    x = dpp_add<0x118, 0xf>(x);
    x = dpp_add<0x142, 0xa>(x);
    x = dpp_add<0x143, 0xc>(x);
    const int n = __builtin_amdgcn_readlane((int)x, 63);
    cnt[lane] = x - c;
    u32 pos[17];
#pragma unroll
    for (int j = 0; j < 17; ++j) pos[j] = *(lds_cu32)(size_t)adr[j];
    __builtin_amdgcn_sched_barrier(0);
#pragma unroll
    for (int i = 0; i < 4; ++i) {
        u32 ca[4], va[4], w[4];
#pragma unroll
        for (int u = 0; u < 4; ++u) {
            const u32 p = pos[4 * i + u] + rank[4 * i + u];
            ca[u] = wbl + 2 * p;
            va[u] = wbl + 4 * p;
            const u32 vv = V[i][u >> 1];
            w[u] = (u & 1) ? ((vv & 0xffff0000u) | rowb[4 * i + u]) : ((vv << 16) | rowb[4 * i + u]);
        }
        asm volatile("s_mov_b64 exec, %16\n\tds_write_b16 %0, %8\n\tds_write_b32 %4, %12 offset:%20\n\t"
                     "s_mov_b64 exec, %17\n\tds_write_b16 %1, %9\n\tds_write_b32 %5, %13 offset:%20\n\t"
                     "s_mov_b64 exec, %18\n\tds_write_b16 %2, %10\n\tds_write_b32 %6, %14 offset:%20\n\t"
                     "s_mov_b64 exec, %19\n\tds_write_b16 %3, %11\n\tds_write_b32 %7, %15 offset:%20\n\t"
                     "s_mov_b64 exec, -1"
                     :
                     : "v"(ca[0]), "v"(ca[1]), "v"(ca[2]), "v"(ca[3]), "v"(va[0]), "v"(va[1]), "v"(va[2]), "v"(va[3]),
                       "v"(I[i][0]), "v"(I[i][1]), "v"(I[i][2]), "v"(I[i][3]), "v"(w[0]), "v"(w[1]), "v"(w[2]), "v"(w[3]),
                       "s"(msk[4 * i]), "s"(msk[4 * i + 1]), "s"(msk[4 * i + 2]), "s"(msk[4 * i + 3]), "i"(OFF_VR)
                     : "memory");
    }
    {
        const u32 p = pos[16] + rank[16];
        const u32 ca = wbl + 2 * p, va = wbl + 4 * p, w = (Vt << 16) | (15u << 4);
        asm volatile("s_mov_b64 exec, %4\n\tds_write_b16 %0, %2\n\tds_write_b32 %1, %3 offset:%5\n\ts_mov_b64 exec, -1"
                     :: "v"(ca), "v"(va), "v"(It), "v"(w), "s"(msk[16]), "i"(OFF_VR) : "memory");
    }

    const u32 laneoff = (lane & 7) * 16;
    const u32 sconst = (lane >> 5) * 1024 + ((lane >> 3) & 3) * 256 + (2 * (lane & 3) + ((lane >> 2) & 1)) * 2;
    const char* colrd = wb + (lane >> 3) * 8;
    f32x4 acc[4];
#pragma unroll
    for (int j = 0; j < 4; ++j) acc[j] = (f32x4){0.f, 0.f, 0.f, 0.f};
    const int nsteps = (n + 31) >> 5;
#define PACE() { \
        u32 a_ = pg; \
        a_ = dpp_min<0x111>(a_); a_ = dpp_min<0x112>(a_); a_ = dpp_min<0x114>(a_); a_ = dpp_min<0x118>(a_); \
        const int ahead_ = __builtin_amdgcn_readlane((int)pg, wib) - __builtin_amdgcn_readlane((int)a_, WPB - 1); \
        if (ahead_ < CFG_M3) __builtin_amdgcn_s_setprio(3); \
        else if (ahead_ < CFG_M2) __builtin_amdgcn_s_setprio(2); \
        else if (ahead_ < CFG_M1) __builtin_amdgcn_s_setprio(1); \
        else __builtin_amdgcn_s_setprio(0); }
#define RDCOLS(s) (*(const u32x2*)(colrd + (s) * 64))
#define BUILD_S(bb) { const u32 sa_ = sconst + (wv & 0xffffu); const bool ok_ = (bb) * 64 + lane < n; \
        if (ok_) *(u16*)(simg + sa_) = (u16)(wv >> 16); \
        S0 = *(const u32x4*)(simg + lane * 16); S1 = *(const u32x4*)(simg + 1024 + lane * 16); \
        if (ok_) *(u16*)(simg + sa_) = 0; \
        wv = vr[((bb) + 1) * 64 + lane]; }
    if (nsteps > 0) {
        u32x4 gA[4], gB[4];
        u32x4 S0, S1;
        const u32x2 c0 = RDCOLS(0);
        u32x2 cB = RDCOLS(1), cA = RDCOLS(2);
        u32 wv = vr[lane];
        u32 pg = progw[lane & (WPB - 1)];
        PACE();
        issue_gather(gA, B, c0, laneoff);
        __builtin_amdgcn_sched_barrier(0);
        BUILD_S(0);
        int b = 0;
        for (; 2 * b + 2 < nsteps; ++b) {
            PACE();
            issue_gather(gB, B, cB, laneoff);
            __builtin_amdgcn_sched_barrier(0);
            compute_step(gA, S0, acc);
            __builtin_amdgcn_sched_barrier(0);
            issue_gather(gA, B, cA, laneoff);
            __builtin_amdgcn_sched_barrier(0);
            compute_step(gB, S1, acc);
            __builtin_amdgcn_sched_barrier(0);
            if (lane == 0) progw[wib] = (cA[0] + rot) & 0xffffu;
            pg = progw[lane & (WPB - 1)];
            cB = RDCOLS(2 * b + 3);
            cA = RDCOLS(2 * b + 4);
            BUILD_S(b + 1);
            __builtin_amdgcn_sched_barrier(0);
        }
        if (2 * b + 1 < nsteps) {
            issue_gather(gB, B, cB, laneoff);
            __builtin_amdgcn_sched_barrier(0);
            compute_step(gA, S0, acc);
            compute_step(gB, S1, acc);
        } else {
            compute_step(gA, S0, acc);
        }
    }
    if (lane == 0) progw[wib] = 0x7fffffffu;
    bool bad = false;
#pragma unroll
    for (int j = 0; j < 4; ++j)
#pragma unroll
        for (int r = 0; r < 4; ++r) bad |= __builtin_amdgcn_classf(acc[j][r], 0x207);
    if (__builtin_expect(__builtin_amdgcn_ballot_w64(bad) != 0, 0)) {
        rows_exact(vals, idx, (const u16*)B, (u16*)out, (size_t)tile * ROWS, ROWS, KS, lane);
    } else {
        u32x2* op = out + ((size_t)tile * ROWS + 4 * (lane >> 4)) * 16 + (lane & 7) * 2 + ((lane >> 3) & 1);
#pragma unroll
        for (int r = 0; r < 4; ++r) {
            u32x2 o;
            o[0] = cvt_pk_bf16(acc[0][r], acc[1][r]);
            o[1] = cvt_pk_bf16(acc[2][r], acc[3][r]);
            __builtin_nontemporal_store(o, op + r * 16);
        }
    }
}

__global__ __launch_bounds__(256) void spmm_ell_generic(const u16* __restrict__ vals, const u32* __restrict__ idx,
                                                        const u16* __restrict__ B, u16* __restrict__ out, int M, int K) {
    const int row = blockIdx.x * 4 + (threadIdx.x >> 6);
    if (row < M) rows_exact(vals, idx, B, out, (size_t)row, 1, K, threadIdx.x & 63);
}

extern "C" void kernel_launch(void* const* d_in, const int* in_sizes, int n_in,
                              void* d_out, int out_size, void* d_ws, size_t ws_size,
                              hipStream_t stream) {
    const u16* vals = (const u16*)d_in[0];
    const u32* idx = (const u32*)d_in[1];
    const char* B = (const char*)d_in[2];
    const int M = out_size / DD;
    const int K = in_sizes[0] / M;
    const int N = in_sizes[2] / DD;
    if (K == KS && N == 65536 && M % (ROWS * WPB) == 0) {
        spmm_ell_kernel<<<M / (ROWS * WPB), WPB * 64, 0, stream>>>(vals, idx, B, (u32x2*)d_out
            );
    } else {
        spmm_ell_generic<<<(M + 3) / 4, 256, 0, stream>>>(vals, idx, (const u16*)B, (u16*)d_out, M, K);
    }
}
```

```cpp
#include <hip/hip_runtime.h>
#include <stdint.h>

typedef unsigned int u32;
typedef unsigned short u16;
typedef unsigned long long u64;
typedef u32 u32x4 __attribute__((ext_vector_type(4)));
typedef u32 u32x2 __attribute__((ext_vector_type(2)));
typedef float f32x4 __attribute__((ext_vector_type(4)));
typedef float f32x2 __attribute__((ext_vector_type(2)));
typedef __bf16 bf16x8 __attribute__((ext_vector_type(8)));
typedef __bf16 bf16x2 __attribute__((ext_vector_type(2)));

constexpr int KS = 65;
constexpr int DD = 64;
constexpr int ROWS = 16;
constexpr int SLOTS = ROWS * KS;
constexpr int LOGP = 6;
constexpr int WPB = 16;
constexpr int COLS_B = 40 * 64;
constexpr int VR_B = 19 * 256;
constexpr int S_B = 2048;
constexpr int CNT_B = 256;
constexpr int WAVE_B = COLS_B + VR_B + S_B + CNT_B;
constexpr int OFF_VR = COLS_B, OFF_S = COLS_B + VR_B, OFF_CNT = COLS_B + VR_B + S_B;

typedef __attribute__((address_space(3))) char* lds_ptr;
typedef __attribute__((address_space(3))) const u32* lds_cu32;

__device__ __forceinline__ u32 cvt_pk_bf16(float a, float b) {
    f32x2 v = {a, b};
    return __builtin_bit_cast(u32, __builtin_convertvector(v, bf16x2));
}
template <int CTRL, int RM>
__device__ __forceinline__ u32 dpp_add(u32 x) { return x + (u32)__builtin_amdgcn_update_dpp(0, (int)x, CTRL, RM, 0xf, true); }
template <int CTRL>
__device__ __forceinline__ u32 dpp_min(u32 x) { return min(x, (u32)__builtin_amdgcn_update_dpp(0x7fffffff, (int)x, CTRL, 0xf, 0xf, false)); }

__device__ __forceinline__ void compute_step(const u32x4 (&g)[4], const u32x4 S, f32x4 (&acc)[4]) {
    const u64 lomask = 0x00ff00ff00ff00ffull;
    u32x4 F0, F1, F2, F3;
#pragma unroll
    for (int t = 0; t < 4; ++t) {
        u32 X0, X1, Y0, Y1;
        asm("s_mov_b64 vcc, %8\n\t"
            "v_cndmask_b32_dpp %0, %6, %4, vcc row_ror:8 row_mask:0xf bank_mask:0xf\n\t"
            "v_cndmask_b32_dpp %1, %7, %5, vcc row_ror:8 row_mask:0xf bank_mask:0xf\n\t"
            "s_not_b64 vcc, vcc\n\t"
            "v_cndmask_b32_dpp %2, %4, %6, vcc row_ror:8 row_mask:0xf bank_mask:0xf\n\t"
            "v_cndmask_b32_dpp %3, %5, %7, vcc row_ror:8 row_mask:0xf bank_mask:0xf"
            : "=&v"(X0), "=&v"(X1), "=&v"(Y0), "=&v"(Y1)
            : "v"(g[t][0]), "v"(g[t][1]), "v"(g[t][2]), "v"(g[t][3]), "s"(lomask)
            : "vcc");
        F0[t] = __builtin_amdgcn_perm(Y0, X0, 0x05040100u);
        F1[t] = __builtin_amdgcn_perm(Y0, X0, 0x07060302u);
        F2[t] = __builtin_amdgcn_perm(Y1, X1, 0x05040100u);
        F3[t] = __builtin_amdgcn_perm(Y1, X1, 0x07060302u);
    }
    const bf16x8 a = __builtin_bit_cast(bf16x8, S);
    acc[0] = __builtin_amdgcn_mfma_f32_16x16x32_bf16(a, __builtin_bit_cast(bf16x8, F0), acc[0], 0, 0, 0);
    acc[1] = __builtin_amdgcn_mfma_f32_16x16x32_bf16(a, __builtin_bit_cast(bf16x8, F1), acc[1], 0, 0, 0);
    acc[2] = __builtin_amdgcn_mfma_f32_16x16x32_bf16(a, __builtin_bit_cast(bf16x8, F2), acc[2], 0, 0, 0);
    acc[3] = __builtin_amdgcn_mfma_f32_16x16x32_bf16(a, __builtin_bit_cast(bf16x8, F3), acc[3], 0, 0, 0);
}

__device__ __forceinline__ void issue_gather(u32x4 (&g)[4], const char* __restrict__ B, u32x2 c, u32 laneoff) {
    u32 o0, o1, o2, o3;
    asm("v_mad_u32_u16 %0, %1, %2, %3" : "=v"(o0) : "v"(c[0]), "s"(128), "v"(laneoff));
    asm("v_mad_u32_u16 %0, %1, %2, %3 op_sel:[1,0,0,0]" : "=v"(o1) : "v"(c[0]), "s"(128), "v"(laneoff));
    asm("v_mad_u32_u16 %0, %1, %2, %3" : "=v"(o2) : "v"(c[1]), "s"(128), "v"(laneoff));
    asm("v_mad_u32_u16 %0, %1, %2, %3 op_sel:[1,0,0,0]" : "=v"(o3) : "v"(c[1]), "s"(128), "v"(laneoff));
    g[0] = *(const u32x4*)(B + o0);
    g[1] = *(const u32x4*)(B + o1);
    g[2] = *(const u32x4*)(B + o2);
    g[3] = *(const u32x4*)(B + o3);
}

__device__ __forceinline__ void rows_exact(const u16* __restrict__ vals, const u32* __restrict__ idx, const u16* __restrict__ B,
                                           u16* __restrict__ out, size_t row0, int nrows, int K, int lane) {
    for (int r = 0; r < nrows; ++r) {
        const size_t m = row0 + r;
        float a = 0.f;
        for (int k = 0; k < K; ++k) {
            const float v = __uint_as_float((u32)vals[m * K + k] << 16);
            const float b = __uint_as_float((u32)B[(size_t)idx[m * K + k] * DD + lane] << 16);
            a = __builtin_fmaf(v, b, a);
        }
        out[m * DD + lane] = (u16)cvt_pk_bf16(a, a);
    }
}

__global__ __launch_bounds__(WPB * 64) void spmm_ell_kernel(const u16* __restrict__ vals, const u32* __restrict__ idx,
                                                            const char* __restrict__ B, u32x2* __restrict__ out) {
    __shared__ __attribute__((aligned(16))) char smem[WPB * WAVE_B + 64];
    const int lane = threadIdx.x & 63;
    const int wib = __builtin_amdgcn_readfirstlane(threadIdx.x >> 6);
    const u32 tile = blockIdx.x * WPB + wib;
    __builtin_amdgcn_s_setprio(3);
    const u32* ip = idx + (size_t)tile * SLOTS;
    const u16* vp = vals + (size_t)tile * SLOTS;
    u32x4 I[4]; u32x2 V[4];
#pragma unroll
    for (int i = 0; i < 4; ++i) {
        V[i] = __builtin_nontemporal_load((const u32x2*)(vp + 256 * i + 4 * lane));
        I[i] = __builtin_nontemporal_load((const u32x4*)(ip + 256 * i + 4 * lane));
    }
    const u32 Vt = __builtin_nontemporal_load(vp + 1024 + (lane & 15));
    const u32 It = __builtin_nontemporal_load(ip + 1024 + (lane & 15));

    const u32 rot = (65536u - (blockIdx.x & 7u) * 8192u) & 0xffffu;
    u32* progw = (u32*)(smem + WPB * WAVE_B);
    if (lane == 0) progw[wib] = 0;
    char* wb = smem + wib * WAVE_B;
    u32* vr = (u32*)(wb + OFF_VR);
    char* simg = wb + OFF_S;
    u32* cnt = (u32*)(wb + OFF_CNT);
    {
        const u32x4 z = {0, 0, 0, 0};
        *(u32x4*)(simg + lane * 16) = z;
        *(u32x4*)(simg + 1024 + lane * 16) = z;
        const u32 padc = ((0xffffu - rot) & 0xffffu) * 0x10001u;
        const u32x4 f = {padc, padc, padc, padc};
        *(u32x4*)(wb + lane * 16) = f;
        *(u32x4*)(wb + 1024 + lane * 16) = f;
        if (lane < (COLS_B - 2048) / 16) *(u32x4*)(wb + 2048 + lane * 16) = f;
        cnt[lane] = 0;
    }
    u32 rowb[16];
#pragma unroll
    for (int i = 0; i < 4; ++i)
#pragma unroll
        for (int u = 0; u < 4; ++u) {
            rowb[4 * i + u] = (((256 * i + 4 * lane + u) * 4033u) >> 14) & 0xf0u;
            asm volatile("" : "+v"(rowb[4 * i + u]));
        }
    const u32 cntb = (u32)(size_t)(lds_ptr)(char*)cnt;
    const u32 wbl = (u32)(size_t)(lds_ptr)wb;
    const u32 vzero = 0, vone = 1;
    u64 msk[17];
    u32 adr[17], rank[17];
#pragma unroll
    for (int i = 0; i < 4; ++i) {
        asm("v_cmp_neq_f16_sdwa %0, %1, %2 src0_sel:WORD_0 src1_sel:WORD_0" : "=s"(msk[4 * i + 0]) : "v"(V[i][0]), "v"(vzero));
        asm("v_cmp_neq_f16_sdwa %0, %1, %2 src0_sel:WORD_1 src1_sel:WORD_0" : "=s"(msk[4 * i + 1]) : "v"(V[i][0]), "v"(vzero));
        asm("v_cmp_neq_f16_sdwa %0, %1, %2 src0_sel:WORD_0 src1_sel:WORD_0" : "=s"(msk[4 * i + 2]) : "v"(V[i][1]), "v"(vzero));
        asm("v_cmp_neq_f16_sdwa %0, %1, %2 src0_sel:WORD_1 src1_sel:WORD_0" : "=s"(msk[4 * i + 3]) : "v"(V[i][1]), "v"(vzero));
#pragma unroll
        for (int u = 0; u < 4; ++u) adr[4 * i + u] = cntb + ((((I[i][u] + rot) >> (16 - LOGP)) & ((1u << LOGP) - 1)) << 2);
        asm volatile("s_nop 0\n\t"
                     "s_mov_b64 exec, %8\n\tds_add_rtn_u32 %0, %4, %12\n\t"
                     "s_mov_b64 exec, %9\n\tds_add_rtn_u32 %1, %5, %12\n\t"
                     "s_mov_b64 exec, %10\n\tds_add_rtn_u32 %2, %6, %12\n\t"
                     "s_mov_b64 exec, %11\n\tds_add_rtn_u32 %3, %7, %12\n\t"
                     "s_mov_b64 exec, -1"
                     : "=&v"(rank[4 * i]), "=&v"(rank[4 * i + 1]), "=&v"(rank[4 * i + 2]), "=&v"(rank[4 * i + 3])
                     : "v"(adr[4 * i]), "v"(adr[4 * i + 1]), "v"(adr[4 * i + 2]), "v"(adr[4 * i + 3]),
                       "s"(msk[4 * i]), "s"(msk[4 * i + 1]), "s"(msk[4 * i + 2]), "s"(msk[4 * i + 3]), "v"(vone)
                     : "memory");
    }
    {
        u64 m;
        asm("v_cmp_neq_f16_sdwa %0, %1, %2 src0_sel:WORD_0 src1_sel:WORD_0" : "=s"(m) : "v"(Vt), "v"(vzero));
        msk[16] = m & 0xffffull;
        adr[16] = cntb + ((((It + rot) >> (16 - LOGP)) & ((1u << LOGP) - 1)) << 2);
        asm volatile("s_nop 0\n\ts_mov_b64 exec, %2\n\tds_add_rtn_u32 %0, %1, %3\n\ts_mov_b64 exec, -1"
                     : "=&v"(rank[16]) : "v"(adr[16]), "s"(msk[16]), "v"(vone) : "memory");
    }
    const u32 c = cnt[lane];
    u32 x = c;
    x = dpp_add<0x111, 0xf>(x);
    asm volatile("s_waitcnt lgkmcnt(0)" ::: "memory");
    x = dpp_add<0x112, 0xf>(x);
    x = dpp_add<0x114, 0xf>(x);
    x = dpp_add<0x118, 0xf>(x);
    x = dpp_add<0x142, 0xa>(x);
    x = dpp_add<0x143, 0xc>(x);
    const int n = __builtin_amdgcn_readlane((int)x, 63);
    cnt[lane] = x - c;
    u32 pos[17];
#pragma unroll
    for (int j = 0; j < 17; ++j) pos[j] = *(lds_cu32)(size_t)adr[j];
    __builtin_amdgcn_sched_barrier(0);
#pragma unroll
    for (int i = 0; i < 4; ++i) {
        u32 ca[4], va[4], w[4];
#pragma unroll
        for (int u = 0; u < 4; ++u) {
            const u32 p = pos[4 * i + u] + rank[4 * i + u];
            ca[u] = wbl + 2 * p;
            va[u] = wbl + 4 * p;
            const u32 vv = V[i][u >> 1];
            w[u] = (u & 1) ? ((vv & 0xffff0000u) | rowb[4 * i + u]) : ((vv << 16) | rowb[4 * i + u]);
        }
        asm volatile("s_mov_b64 exec, %16\n\tds_write_b16 %0, %8\n\tds_write_b32 %4, %12 offset:%20\n\t"
                     "s_mov_b64 exec, %17\n\tds_write_b16 %1, %9\n\tds_write_b32 %5, %13 offset:%20\n\t"
                     "s_mov_b64 exec, %18\n\tds_write_b16 %2, %10\n\tds_write_b32 %6, %14 offset:%20\n\t"
                     "s_mov_b64 exec, %19\n\tds_write_b16 %3, %11\n\tds_write_b32 %7, %15 offset:%20\n\t"
                     "s_mov_b64 exec, -1"
                     :
                     : "v"(ca[0]), "v"(ca[1]), "v"(ca[2]), "v"(ca[3]), "v"(va[0]), "v"(va[1]), "v"(va[2]), "v"(va[3]),
                       "v"(I[i][0]), "v"(I[i][1]), "v"(I[i][2]), "v"(I[i][3]), "v"(w[0]), "v"(w[1]), "v"(w[2]), "v"(w[3]),
                       "s"(msk[4 * i]), "s"(msk[4 * i + 1]), "s"(msk[4 * i + 2]), "s"(msk[4 * i + 3]), "i"(OFF_VR)
                     : "memory");
    }
    {
        const u32 p = pos[16] + rank[16];
        const u32 ca = wbl + 2 * p, va = wbl + 4 * p, w = (Vt << 16) | (15u << 4);
        asm volatile("s_mov_b64 exec, %4\n\tds_write_b16 %0, %2\n\tds_write_b32 %1, %3 offset:%5\n\ts_mov_b64 exec, -1"
                     :: "v"(ca), "v"(va), "v"(It), "v"(w), "s"(msk[16]), "i"(OFF_VR) : "memory");
    }

    const u32 laneoff = (lane & 7) * 16;
    const u32 sconst = (lane >> 5) * 1024 + ((lane >> 3) & 3) * 256 + (2 * (lane & 3) + ((lane >> 2) & 1)) * 2;
    const char* colrd = wb + (lane >> 3) * 8;
    f32x4 acc[4];
#pragma unroll
    for (int j = 0; j < 4; ++j) acc[j] = (f32x4){0.f, 0.f, 0.f, 0.f};
    const int nsteps = (n + 31) >> 5;
#define PACE() { \
        u32 a_ = pg; \
        a_ = dpp_min<0x111>(a_); a_ = dpp_min<0x112>(a_); a_ = dpp_min<0x114>(a_); a_ = dpp_min<0x118>(a_); \
        const int ahead_ = __builtin_amdgcn_readlane((int)pg, wib) - __builtin_amdgcn_readlane((int)a_, WPB - 1); \
        if (ahead_ < 4000) __builtin_amdgcn_s_setprio(3); \
        else if (ahead_ < 8000) __builtin_amdgcn_s_setprio(2); \
        else if (ahead_ < 12000) __builtin_amdgcn_s_setprio(1); \
        else __builtin_amdgcn_s_setprio(0); }
#define RDCOLS(s) (*(const u32x2*)(colrd + (s) * 64))
#define BUILD_S(bb) { const u32 sa_ = sconst + (wv & 0xffffu); const bool ok_ = (bb) * 64 + lane < n; \
        if (ok_) *(u16*)(simg + sa_) = (u16)(wv >> 16); \
        S0 = *(const u32x4*)(simg + lane * 16); S1 = *(const u32x4*)(simg + 1024 + lane * 16); \
        if (ok_) *(u16*)(simg + sa_) = 0; \
        wv = vr[((bb) + 1) * 64 + lane]; }
    if (nsteps > 0) {
        u32x4 gA[4], gB[4];
        u32x4 S0, S1;
        const u32x2 c0 = RDCOLS(0);
        u32x2 cB = RDCOLS(1), cA = RDCOLS(2);
        u32 wv = vr[lane];
        u32 pg = progw[lane & (WPB - 1)];
        PACE();
        issue_gather(gA, B, c0, laneoff);
        __builtin_amdgcn_sched_barrier(0);
        BUILD_S(0);
        int b = 0;
        for (; 2 * b + 2 < nsteps; ++b) {
            PACE();
            issue_gather(gB, B, cB, laneoff);
            __builtin_amdgcn_sched_barrier(0);
            compute_step(gA, S0, acc);
            __builtin_amdgcn_sched_barrier(0);
            issue_gather(gA, B, cA, laneoff);
            __builtin_amdgcn_sched_barrier(0);
            compute_step(gB, S1, acc);
            __builtin_amdgcn_sched_barrier(0);
            if (lane == 0) progw[wib] = (cA[0] + rot) & 0xffffu;
            pg = progw[lane & (WPB - 1)];
            cB = RDCOLS(2 * b + 3);
            cA = RDCOLS(2 * b + 4);
            BUILD_S(b + 1);
            __builtin_amdgcn_sched_barrier(0);
        }
        if (2 * b + 1 < nsteps) {
            issue_gather(gB, B, cB, laneoff);
            __builtin_amdgcn_sched_barrier(0);
            compute_step(gA, S0, acc);
            compute_step(gB, S1, acc);
        } else {
            compute_step(gA, S0, acc);
        }
    }
    if (lane == 0) progw[wib] = 0x7fffffffu;
    bool bad = false;
#pragma unroll
    for (int j = 0; j < 4; ++j)
#pragma unroll
        for (int r = 0; r < 4; ++r) bad |= __builtin_amdgcn_classf(acc[j][r], 0x207);
    if (__builtin_expect(__builtin_amdgcn_ballot_w64(bad) != 0, 0)) {
        rows_exact(vals, idx, (const u16*)B, (u16*)out, (size_t)tile * ROWS, ROWS, KS, lane);
    } else {
        u32x2* op = out + ((size_t)tile * ROWS + 4 * (lane >> 4)) * 16 + (lane & 7) * 2 + ((lane >> 3) & 1);
#pragma unroll
        for (int r = 0; r < 4; ++r) {
            u32x2 o;
            o[0] = cvt_pk_bf16(acc[0][r], acc[1][r]);
            o[1] = cvt_pk_bf16(acc[2][r], acc[3][r]);
            __builtin_nontemporal_store(o, op + r * 16);
        }
    }
}

__global__ __launch_bounds__(256) void spmm_ell_generic(const u16* __restrict__ vals, const u32* __restrict__ idx,
                                                        const u16* __restrict__ B, u16* __restrict__ out, int M, int K) {
    const int row = blockIdx.x * 4 + (threadIdx.x >> 6);
    if (row < M) rows_exact(vals, idx, B, out, (size_t)row, 1, K, threadIdx.x & 63);
}

extern "C" void kernel_launch(void* const* d_in, const int* in_sizes, int n_in,
                              void* d_out, int out_size, void* d_ws, size_t ws_size,
                              hipStream_t stream) {
    const u16* vals = (const u16*)d_in[0];
    const u32* idx = (const u32*)d_in[1];
    const char* B = (const char*)d_in[2];
    const int M = out_size / DD;
    const int K = in_sizes[0] / M;
    const int N = in_sizes[2] / DD;
    if (K == KS && N == 65536 && M % (ROWS * WPB) == 0) {
        spmm_ell_kernel<<<M / (ROWS * WPB), WPB * 64, 0, stream>>>(vals, idx, B, (u32x2*)d_out);
    } else {
        spmm_ell_generic<<<(M + 3) / 4, 256, 0, stream>>>(vals, idx, (const u16*)B, (u16*)d_out, M, K);
    }
}
```

```cpp
#include <hip/hip_runtime.h>
#include <stdint.h>

typedef unsigned int u32;
typedef unsigned short u16;
typedef unsigned long long u64;
typedef u32 u32x4 __attribute__((ext_vector_type(4)));
typedef u32 u32x2 __attribute__((ext_vector_type(2)));
typedef float f32x4 __attribute__((ext_vector_type(4)));
typedef float f32x2 __attribute__((ext_vector_type(2)));
typedef __bf16 bf16x8 __attribute__((ext_vector_type(8)));
typedef __bf16 bf16x2 __attribute__((ext_vector_type(2)));

constexpr int KS = 65;
constexpr int DD = 64;
constexpr int ROWS = 16;
constexpr int SLOTS = ROWS * KS;
constexpr int LOGP = 6;
constexpr int WPB = 16;
constexpr int COLS_B = 40 * 64;
constexpr int VR_B = 19 * 256;
constexpr int S_B = 2048;
constexpr int CNT_B = 256;
constexpr int WAVE_B = COLS_B + VR_B + S_B + CNT_B;
constexpr int OFF_VR = COLS_B, OFF_S = COLS_B + VR_B, OFF_CNT = COLS_B + VR_B + S_B;

typedef __attribute__((address_space(3))) char* lds_ptr;
typedef __attribute__((address_space(3))) const u32* lds_cu32;

__device__ __forceinline__ u32 cvt_pk_bf16(float a, float b) {
    f32x2 v = {a, b};
    return __builtin_bit_cast(u32, __builtin_convertvector(v, bf16x2));
}
template <int CTRL, int RM>
__device__ __forceinline__ u32 dpp_add(u32 x) { return x + (u32)__builtin_amdgcn_update_dpp(0, (int)x, CTRL, RM, 0xf, true); }
template <int CTRL>
__device__ __forceinline__ u32 dpp_min(u32 x) { return min(x, (u32)__builtin_amdgcn_update_dpp(0x7fffffff, (int)x, CTRL, 0xf, 0xf, false)); }

__device__ __forceinline__ void compute_step(const u32x4 (&g)[4], const u32x4 S, f32x4 (&acc)[4]) {
    const u64 lomask = 0x00ff00ff00ff00ffull;
    u32x4 F0, F1, F2, F3;
#pragma unroll
    for (int t = 0; t < 4; ++t) {
        u32 X0, X1, Y0, Y1;
        asm("s_mov_b64 vcc, %8\n\t"
            "v_cndmask_b32_dpp %0, %6, %4, vcc row_ror:8 row_mask:0xf bank_mask:0xf\n\t"
            "v_cndmask_b32_dpp %1, %7, %5, vcc row_ror:8 row_mask:0xf bank_mask:0xf\n\t"
            "s_not_b64 vcc, vcc\n\t"
            "v_cndmask_b32_dpp %2, %4, %6, vcc row_ror:8 row_mask:0xf bank_mask:0xf\n\t"
            "v_cndmask_b32_dpp %3, %5, %7, vcc row_ror:8 row_mask:0xf bank_mask:0xf"
            : "=&v"(X0), "=&v"(X1), "=&v"(Y0), "=&v"(Y1)
            : "v"(g[t][0]), "v"(g[t][1]), "v"(g[t][2]), "v"(g[t][3]), "s"(lomask)
            : "vcc");
        F0[t] = __builtin_amdgcn_perm(Y0, X0, 0x05040100u);
        F1[t] = __builtin_amdgcn_perm(Y0, X0, 0x07060302u);
        F2[t] = __builtin_amdgcn_perm(Y1, X1, 0x05040100u);
        F3[t] = __builtin_amdgcn_perm(Y1, X1, 0x07060302u);
    }
    const bf16x8 a = __builtin_bit_cast(bf16x8, S);
    acc[0] = __builtin_amdgcn_mfma_f32_16x16x32_bf16(a, __builtin_bit_cast(bf16x8, F0), acc[0], 0, 0, 0);
    acc[1] = __builtin_amdgcn_mfma_f32_16x16x32_bf16(a, __builtin_bit_cast(bf16x8, F1), acc[1], 0, 0, 0);
    acc[2] = __builtin_amdgcn_mfma_f32_16x16x32_bf16(a, __builtin_bit_cast(bf16x8, F2), acc[2], 0, 0, 0);
    acc[3] = __builtin_amdgcn_mfma_f32_16x16x32_bf16(a, __builtin_bit_cast(bf16x8, F3), acc[3], 0, 0, 0);
}

__device__ __forceinline__ void issue_gather(u32x4 (&g)[4], const char* __restrict__ B, u32x2 c, u32 laneoff) {
    u32 o0, o1, o2, o3;
    asm("v_mad_u32_u16 %0, %1, %2, %3" : "=v"(o0) : "v"(c[0]), "s"(128), "v"(laneoff));
    asm("v_mad_u32_u16 %0, %1, %2, %3 op_sel:[1,0,0,0]" : "=v"(o1) : "v"(c[0]), "s"(128), "v"(laneoff));
    asm("v_mad_u32_u16 %0, %1, %2, %3" : "=v"(o2) : "v"(c[1]), "s"(128), "v"(laneoff));
    asm("v_mad_u32_u16 %0, %1, %2, %3 op_sel:[1,0,0,0]" : "=v"(o3) : "v"(c[1]), "s"(128), "v"(laneoff));
    g[0] = *(const u32x4*)(B + o0);
    g[1] = *(const u32x4*)(B + o1);
    g[2] = *(const u32x4*)(B + o2);
    g[3] = *(const u32x4*)(B + o3);
}

__device__ __forceinline__ void rows_exact(const u16* __restrict__ vals, const u32* __restrict__ idx, const u16* __restrict__ B,
                                           u16* __restrict__ out, size_t row0, int nrows, int K, int lane) {
    for (int r = 0; r < nrows; ++r) {
        const size_t m = row0 + r;
        float a = 0.f;
        for (int k = 0; k < K; ++k) {
            const float v = __uint_as_float((u32)vals[m * K + k] << 16);
            const float b = __uint_as_float((u32)B[(size_t)idx[m * K + k] * DD + lane] << 16);
            a = __builtin_fmaf(v, b, a);
        }
        out[m * DD + lane] = (u16)cvt_pk_bf16(a, a);
    }
}

__global__ __launch_bounds__(WPB * 64) void spmm_ell_kernel(const u16* __restrict__ vals, const u32* __restrict__ idx,
                                                            const char* __restrict__ B, u32x2* __restrict__ out) {
    __shared__ __attribute__((aligned(16))) char smem[WPB * WAVE_B + 64];
    const int lane = threadIdx.x & 63;
    const int wib = __builtin_amdgcn_readfirstlane(threadIdx.x >> 6);
    const u32 tile = blockIdx.x * WPB + wib;
    __builtin_amdgcn_s_setprio(3);
    const u32* ip = idx + (size_t)tile * SLOTS;
    const u16* vp = vals + (size_t)tile * SLOTS;
    u32x4 I[4]; u32x2 V[4];
#pragma unroll
    for (int i = 0; i < 4; ++i) {
        V[i] = __builtin_nontemporal_load((const u32x2*)(vp + 256 * i + 4 * lane));
        I[i] = __builtin_nontemporal_load((const u32x4*)(ip + 256 * i + 4 * lane));
    }
    const u32 Vt = __builtin_nontemporal_load(vp + 1024 + (lane & 15));
    const u32 It = __builtin_nontemporal_load(ip + 1024 + (lane & 15));

    const u32 rot = (65536u - (blockIdx.x & 7u) * 8192u) & 0xffffu;
    u32* progw = (u32*)(smem + WPB * WAVE_B);
    if (lane == 0) progw[wib] = 0;
    char* wb = smem + wib * WAVE_B;
    u32* vr = (u32*)(wb + OFF_VR);
    char* simg = wb + OFF_S;
    u32* cnt = (u32*)(wb + OFF_CNT);
    {
        const u32x4 z = {0, 0, 0, 0};
        *(u32x4*)(simg + lane * 16) = z;
        *(u32x4*)(simg + 1024 + lane * 16) = z;
        const u32 padc = ((0xffffu - rot) & 0xffffu) * 0x10001u;
        const u32x4 f = {padc, padc, padc, padc};
        *(u32x4*)(wb + lane * 16) = f;
        *(u32x4*)(wb + 1024 + lane * 16) = f;
        if (lane < (COLS_B - 2048) / 16) *(u32x4*)(wb + 2048 + lane * 16) = f;
        cnt[lane] = 0;
    }
    u32 rowb[16];
#pragma unroll
    for (int i = 0; i < 4; ++i)
#pragma unroll
        for (int u = 0; u < 4; ++u) {
            rowb[4 * i + u] = (((256 * i + 4 * lane + u) * 4033u) >> 14) & 0xf0u;
            asm volatile("" : "+v"(rowb[4 * i + u]));
        }
    const u32 cntb = (u32)(size_t)(lds_ptr)(char*)cnt;
    const u32 wbl = (u32)(size_t)(lds_ptr)wb;
    const u32 vzero = 0, vone = 1;
    u64 msk[17];
    u32 adr[17], rank[17];
#pragma unroll
    for (int i = 0; i < 4; ++i) {
        asm("v_cmp_neq_f16_sdwa %0, %1, %2 src0_sel:WORD_0 src1_sel:WORD_0" : "=s"(msk[4 * i + 0]) : "v"(V[i][0]), "v"(vzero));
        asm("v_cmp_neq_f16_sdwa %0, %1, %2 src0_sel:WORD_1 src1_sel:WORD_0" : "=s"(msk[4 * i + 1]) : "v"(V[i][0]), "v"(vzero));
        asm("v_cmp_neq_f16_sdwa %0, %1, %2 src0_sel:WORD_0 src1_sel:WORD_0" : "=s"(msk[4 * i + 2]) : "v"(V[i][1]), "v"(vzero));
        asm("v_cmp_neq_f16_sdwa %0, %1, %2 src0_sel:WORD_1 src1_sel:WORD_0" : "=s"(msk[4 * i + 3]) : "v"(V[i][1]), "v"(vzero));
#pragma unroll
        for (int u = 0; u < 4; ++u) adr[4 * i + u] = cntb + ((((I[i][u] + rot) >> (16 - LOGP)) & ((1u << LOGP) - 1)) << 2);
        asm volatile("s_nop 0\n\t"
                     "s_mov_b64 exec, %8\n\tds_add_rtn_u32 %0, %4, %12\n\t"
                     "s_mov_b64 exec, %9\n\tds_add_rtn_u32 %1, %5, %12\n\t"
                     "s_mov_b64 exec, %10\n\tds_add_rtn_u32 %2, %6, %12\n\t"
                     "s_mov_b64 exec, %11\n\tds_add_rtn_u32 %3, %7, %12\n\t"
                     "s_mov_b64 exec, -1"
                     : "=&v"(rank[4 * i]), "=&v"(rank[4 * i + 1]), "=&v"(rank[4 * i + 2]), "=&v"(rank[4 * i + 3])
                     : "v"(adr[4 * i]), "v"(adr[4 * i + 1]), "v"(adr[4 * i + 2]), "v"(adr[4 * i + 3]),
                       "s"(msk[4 * i]), "s"(msk[4 * i + 1]), "s"(msk[4 * i + 2]), "s"(msk[4 * i + 3]), "v"(vone)
                     : "memory");
    }
    {
        u64 m;
        asm("v_cmp_neq_f16_sdwa %0, %1, %2 src0_sel:WORD_0 src1_sel:WORD_0" : "=s"(m) : "v"(Vt), "v"(vzero));
        msk[16] = m & 0xffffull;
        adr[16] = cntb + ((((It + rot) >> (16 - LOGP)) & ((1u << LOGP) - 1)) << 2);
        asm volatile("s_nop 0\n\ts_mov_b64 exec, %2\n\tds_add_rtn_u32 %0, %1, %3\n\ts_mov_b64 exec, -1"
                     : "=&v"(rank[16]) : "v"(adr[16]), "s"(msk[16]), "v"(vone) : "memory");
    }
    const u32 c = cnt[lane];
    u32 x = c;
    x = dpp_add<0x111, 0xf>(x);
    asm volatile("s_waitcnt lgkmcnt(0)" ::: "memory");
    x = dpp_add<0x112, 0xf>(x);
    x = dpp_add<0x114, 0xf>(x);
    x = dpp_add<0x118, 0xf>(x);
    x = dpp_add<0x142, 0xa>(x);
    x = dpp_add<0x143, 0xc>(x);
    const int n = __builtin_amdgcn_readlane((int)x, 63);
    cnt[lane] = x - c;
    u32 pos[17];
#pragma unroll
    for (int j = 0; j < 17; ++j) pos[j] = *(lds_cu32)(size_t)adr[j];
    __builtin_amdgcn_sched_barrier(0);
#pragma unroll
    for (int i = 0; i < 4; ++i) {
        u32 ca[4], va[4], w[4];
#pragma unroll
        for (int u = 0; u < 4; ++u) {
            const u32 p = pos[4 * i + u] + rank[4 * i + u];
            ca[u] = wbl + 2 * p;
            va[u] = wbl + 4 * p;
            const u32 vv = V[i][u >> 1];
            w[u] = (u & 1) ? ((vv & 0xffff0000u) | rowb[4 * i + u]) : ((vv << 16) | rowb[4 * i + u]);
        }
        asm volatile("s_mov_b64 exec, %16\n\tds_write_b16 %0, %8\n\tds_write_b32 %4, %12 offset:%20\n\t"
                     "s_mov_b64 exec, %17\n\tds_write_b16 %1, %9\n\tds_write_b32 %5, %13 offset:%20\n\t"
                     "s_mov_b64 exec, %18\n\tds_write_b16 %2, %10\n\tds_write_b32 %6, %14 offset:%20\n\t"
                     "s_mov_b64 exec, %19\n\tds_write_b16 %3, %11\n\tds_write_b32 %7, %15 offset:%20\n\t"
                     "s_mov_b64 exec, -1"
                     :
                     : "v"(ca[0]), "v"(ca[1]), "v"(ca[2]), "v"(ca[3]), "v"(va[0]), "v"(va[1]), "v"(va[2]), "v"(va[3]),
                       "v"(I[i][0]), "v"(I[i][1]), "v"(I[i][2]), "v"(I[i][3]), "v"(w[0]), "v"(w[1]), "v"(w[2]), "v"(w[3]),
                       "s"(msk[4 * i]), "s"(msk[4 * i + 1]), "s"(msk[4 * i + 2]), "s"(msk[4 * i + 3]), "i"(OFF_VR)
                     : "memory");
    }
    {
        const u32 p = pos[16] + rank[16];
        const u32 ca = wbl + 2 * p, va = wbl + 4 * p, w = (Vt << 16) | (15u << 4);
        asm volatile("s_mov_b64 exec, %4\n\tds_write_b16 %0, %2\n\tds_write_b32 %1, %3 offset:%5\n\ts_mov_b64 exec, -1"
                     :: "v"(ca), "v"(va), "v"(It), "v"(w), "s"(msk[16]), "i"(OFF_VR) : "memory");
    }

    const u32 laneoff = (lane & 7) * 16;
    const u32 sconst = (lane >> 5) * 1024 + ((lane >> 3) & 3) * 256 + (2 * (lane & 3) + ((lane >> 2) & 1)) * 2;
    const char* colrd = wb + (lane >> 3) * 8;
    f32x4 acc[4];
#pragma unroll
    for (int j = 0; j < 4; ++j) acc[j] = (f32x4){0.f, 0.f, 0.f, 0.f};
    const int nsteps = (n + 31) >> 5;
#define PACE() { \
        u32 a_ = pg; \
        a_ = dpp_min<0x111>(a_); a_ = dpp_min<0x112>(a_); a_ = dpp_min<0x114>(a_); a_ = dpp_min<0x118>(a_); \
        const int min_ = __builtin_amdgcn_readlane((int)a_, WPB - 1); \
        const int ahead_ = __builtin_amdgcn_readlane((int)pg, wib) - min_; \
        if (ahead_ >= 12000) __builtin_amdgcn_s_setprio(0); \
        else if (min_ == 0) { if (ahead_ < 4000) __builtin_amdgcn_s_setprio(2); else __builtin_amdgcn_s_setprio(1); } \
        else if (ahead_ < 4000) __builtin_amdgcn_s_setprio(3); \
        else if (ahead_ < 8000) __builtin_amdgcn_s_setprio(2); \
        else __builtin_amdgcn_s_setprio(1); }
#define RDCOLS(s) (*(const u32x2*)(colrd + (s) * 64))
#define BUILD_S(bb) { const u32 sa_ = sconst + (wv & 0xffffu); const bool ok_ = (bb) * 64 + lane < n; \
        if (ok_) *(u16*)(simg + sa_) = (u16)(wv >> 16); \
        S0 = *(const u32x4*)(simg + lane * 16); S1 = *(const u32x4*)(simg + 1024 + lane * 16); \
        if (ok_) *(u16*)(simg + sa_) = 0; \
        wv = vr[((bb) + 1) * 64 + lane]; }
    if (nsteps > 0) {
        u32x4 gA[4], gB[4];
        u32x4 S0, S1;
        const u32x2 c0 = RDCOLS(0);
        u32x2 cB = RDCOLS(1), cA = RDCOLS(2);
        u32 wv = vr[lane];
        u32 pg = progw[lane & (WPB - 1)];
        PACE();
        issue_gather(gA, B, c0, laneoff);
        __builtin_amdgcn_sched_barrier(0);
        BUILD_S(0);
        int b = 0;
        for (; 2 * b + 2 < nsteps; ++b) {
            PACE();
            issue_gather(gB, B, cB, laneoff);
            __builtin_amdgcn_sched_barrier(0);
            compute_step(gA, S0, acc);
            __builtin_amdgcn_sched_barrier(0);
            issue_gather(gA, B, cA, laneoff);
            __builtin_amdgcn_sched_barrier(0);
            compute_step(gB, S1, acc);
            __builtin_amdgcn_sched_barrier(0);
            if (lane == 0) progw[wib] = (cA[0] + rot) & 0xffffu;
            pg = progw[lane & (WPB - 1)];
            cB = RDCOLS(2 * b + 3);
            cA = RDCOLS(2 * b + 4);
            BUILD_S(b + 1);
            __builtin_amdgcn_sched_barrier(0);
        }
        if (2 * b + 1 < nsteps) {
            issue_gather(gB, B, cB, laneoff);
            __builtin_amdgcn_sched_barrier(0);
            compute_step(gA, S0, acc);
            compute_step(gB, S1, acc);
        } else {
            compute_step(gA, S0, acc);
        }
    }
    if (lane == 0) progw[wib] = 0x7fffffffu;
    bool bad = false;
#pragma unroll
    for (int j = 0; j < 4; ++j)
#pragma unroll
        for (int r = 0; r < 4; ++r) bad |= __builtin_amdgcn_classf(acc[j][r], 0x207);
    if (__builtin_expect(__builtin_amdgcn_ballot_w64(bad) != 0, 0)) {
        rows_exact(vals, idx, (const u16*)B, (u16*)out, (size_t)tile * ROWS, ROWS, KS, lane);
    } else {
        u32x2* op = out + ((size_t)tile * ROWS + 4 * (lane >> 4)) * 16 + (lane & 7) * 2 + ((lane >> 3) & 1);
#pragma unroll
        for (int r = 0; r < 4; ++r) {
            u32x2 o;
            o[0] = cvt_pk_bf16(acc[0][r], acc[1][r]);
            o[1] = cvt_pk_bf16(acc[2][r], acc[3][r]);
            __builtin_nontemporal_store(o, op + r * 16);
        }
    }
}

__global__ __launch_bounds__(256) void spmm_ell_generic(const u16* __restrict__ vals, const u32* __restrict__ idx,
                                                        const u16* __restrict__ B, u16* __restrict__ out, int M, int K) {
    const int row = blockIdx.x * 4 + (threadIdx.x >> 6);
    if (row < M) rows_exact(vals, idx, B, out, (size_t)row, 1, K, threadIdx.x & 63);
}

extern "C" void kernel_launch(void* const* d_in, const int* in_sizes, int n_in,
                              void* d_out, int out_size, void* d_ws, size_t ws_size,
                              hipStream_t stream) {
    const u16* vals = (const u16*)d_in[0];
    const u32* idx = (const u32*)d_in[1];
    const char* B = (const char*)d_in[2];
    const int M = out_size / DD;
    const int K = in_sizes[0] / M;
    const int N = in_sizes[2] / DD;
    if (K == KS && N == 65536 && M % (ROWS * WPB) == 0) {
        spmm_ell_kernel<<<M / (ROWS * WPB), WPB * 64, 0, stream>>>(vals, idx, B, (u32x2*)d_out);
    } else {
        spmm_ell_generic<<<(M + 3) / 4, 256, 0, stream>>>(vals, idx, (const u16*)B, (u16*)d_out, M, K);
    }
}
```

```cpp
#include <hip/hip_runtime.h>
#include <stdint.h>

typedef unsigned int u32;
typedef unsigned short u16;
typedef unsigned long long u64;
typedef u32 u32x4 __attribute__((ext_vector_type(4)));
typedef u32 u32x2 __attribute__((ext_vector_type(2)));
typedef float f32x4 __attribute__((ext_vector_type(4)));
typedef float f32x2 __attribute__((ext_vector_type(2)));
typedef __bf16 bf16x8 __attribute__((ext_vector_type(8)));
typedef __bf16 bf16x2 __attribute__((ext_vector_type(2)));

constexpr int KS = 65;
constexpr int DD = 64;
constexpr int ROWS = 16;
constexpr int SLOTS = ROWS * KS;
constexpr int LOGP = 6;
constexpr int WPB = 16;
constexpr int COLS_B = 40 * 64;
constexpr int VR_B = 19 * 256;
constexpr int S_B = 2048;
constexpr int CNT_B = 256;
constexpr int WAVE_B = COLS_B + VR_B + S_B + CNT_B;
constexpr int OFF_VR = COLS_B, OFF_S = COLS_B + VR_B, OFF_CNT = COLS_B + VR_B + S_B;

typedef __attribute__((address_space(3))) char* lds_ptr;
typedef __attribute__((address_space(3))) const u32* lds_cu32;

__device__ __forceinline__ u32 cvt_pk_bf16(float a, float b) {
    f32x2 v = {a, b};
    return __builtin_bit_cast(u32, __builtin_convertvector(v, bf16x2));
}
template <int CTRL, int RM>
__device__ __forceinline__ u32 dpp_add(u32 x) { return x + (u32)__builtin_amdgcn_update_dpp(0, (int)x, CTRL, RM, 0xf, true); }

__device__ __forceinline__ void compute_step(const u32x4 (&g)[4], const u32x4 S, f32x4 (&acc)[4]) {
    const u64 lomask = 0x00ff00ff00ff00ffull;
    u32x4 F0, F1, F2, F3;
#pragma unroll
    for (int t = 0; t < 4; t += 2) {
        u32 X0, X1, Y0, Y1, Z0, Z1, W0, W1;
        asm("s_mov_b64 vcc, %16\n\ts_nop 1\n\t"
            "v_cndmask_b32_dpp %0, %10, %8, vcc row_ror:8 row_mask:0xf bank_mask:0xf\n\t"
            "v_cndmask_b32_dpp %1, %11, %9, vcc row_ror:8 row_mask:0xf bank_mask:0xf\n\t"
            "v_cndmask_b32_dpp %4, %14, %12, vcc row_ror:8 row_mask:0xf bank_mask:0xf\n\t"
            "v_cndmask_b32_dpp %5, %15, %13, vcc row_ror:8 row_mask:0xf bank_mask:0xf\n\t"
            "s_not_b64 vcc, vcc\n\t"
            "v_cndmask_b32_dpp %2, %8, %10, vcc row_ror:8 row_mask:0xf bank_mask:0xf\n\t"
            "v_cndmask_b32_dpp %3, %9, %11, vcc row_ror:8 row_mask:0xf bank_mask:0xf\n\t"
            "v_cndmask_b32_dpp %6, %12, %14, vcc row_ror:8 row_mask:0xf bank_mask:0xf\n\t"
            "v_cndmask_b32_dpp %7, %13, %15, vcc row_ror:8 row_mask:0xf bank_mask:0xf"
            : "=&v"(X0), "=&v"(X1), "=&v"(Y0), "=&v"(Y1), "=&v"(Z0), "=&v"(Z1), "=&v"(W0), "=&v"(W1)
            : "v"(g[t][0]), "v"(g[t][1]), "v"(g[t][2]), "v"(g[t][3]),
              "v"(g[t + 1][0]), "v"(g[t + 1][1]), "v"(g[t + 1][2]), "v"(g[t + 1][3]), "s"(lomask)
            : "vcc");
        F0[t] = __builtin_amdgcn_perm(Y0, X0, 0x05040100u);
        F1[t] = __builtin_amdgcn_perm(Y0, X0, 0x07060302u);
        F2[t] = __builtin_amdgcn_perm(Y1, X1, 0x05040100u);
        F3[t] = __builtin_amdgcn_perm(Y1, X1, 0x07060302u);
        F0[t + 1] = __builtin_amdgcn_perm(W0, Z0, 0x05040100u);
        F1[t + 1] = __builtin_amdgcn_perm(W0, Z0, 0x07060302u);
        F2[t + 1] = __builtin_amdgcn_perm(W1, Z1, 0x05040100u);
        F3[t + 1] = __builtin_amdgcn_perm(W1, Z1, 0x07060302u);
    }
    const bf16x8 a = __builtin_bit_cast(bf16x8, S);
    acc[0] = __builtin_amdgcn_mfma_f32_16x16x32_bf16(a, __builtin_bit_cast(bf16x8, F0), acc[0], 0, 0, 0);
    acc[1] = __builtin_amdgcn_mfma_f32_16x16x32_bf16(a, __builtin_bit_cast(bf16x8, F1), acc[1], 0, 0, 0);
    acc[2] = __builtin_amdgcn_mfma_f32_16x16x32_bf16(a, __builtin_bit_cast(bf16x8, F2), acc[2], 0, 0, 0);
    acc[3] = __builtin_amdgcn_mfma_f32_16x16x32_bf16(a, __builtin_bit_cast(bf16x8, F3), acc[3], 0, 0, 0);
}

__device__ __forceinline__ void issue_gather(u32x4 (&g)[4], const char* __restrict__ B, u32x2 c, u32 laneoff) {
    u32 o0, o1, o2, o3;
    asm("s_nop 2\n\t"
        "v_mad_u32_u16 %0, %4, %6, %7\n\t"
        "v_mad_u32_u16 %1, %4, %6, %7 op_sel:[1,0,0,0]\n\t"
        "v_mad_u32_u16 %2, %5, %6, %7\n\t"
        "v_mad_u32_u16 %3, %5, %6, %7 op_sel:[1,0,0,0]"
        : "=&v"(o0), "=&v"(o1), "=&v"(o2), "=&v"(o3) : "v"(c[0]), "v"(c[1]), "s"(128), "v"(laneoff));
    g[0] = *(const u32x4*)(B + o0);
    g[1] = *(const u32x4*)(B + o1);
    g[2] = *(const u32x4*)(B + o2);
    g[3] = *(const u32x4*)(B + o3);
}

__device__ __forceinline__ void rows_exact(const u16* __restrict__ vals, const u32* __restrict__ idx, const u16* __restrict__ B,
                                           u16* __restrict__ out, size_t row0, int nrows, int K, int lane) {
    for (int r = 0; r < nrows; ++r) {
        const size_t m = row0 + r;
        float a = 0.f;
        for (int k = 0; k < K; ++k) {
            const float v = __uint_as_float((u32)vals[m * K + k] << 16);
            const float b = __uint_as_float((u32)B[(size_t)idx[m * K + k] * DD + lane] << 16);
            a = __builtin_fmaf(v, b, a);
        }
        out[m * DD + lane] = (u16)cvt_pk_bf16(a, a);
    }
}

__global__ __launch_bounds__(WPB * 64) void spmm_ell_kernel(const u16* __restrict__ vals, const u32* __restrict__ idx,
                                                            const char* __restrict__ B, u32x2* __restrict__ out) {
    __shared__ __attribute__((aligned(16))) char smem[WPB * WAVE_B + 64];
    const int lane = threadIdx.x & 63;
    const int wib = __builtin_amdgcn_readfirstlane(threadIdx.x >> 6);
    const u32 tile = blockIdx.x * WPB + wib;
    __builtin_amdgcn_s_setprio(3);
    const u32* ip = idx + (size_t)tile * SLOTS;
    const u16* vp = vals + (size_t)tile * SLOTS;
    u32x4 I[4]; u32x2 V[4];
#pragma unroll
    for (int i = 0; i < 4; ++i) {
        V[i] = __builtin_nontemporal_load((const u32x2*)(vp + 256 * i + 4 * lane));
        I[i] = __builtin_nontemporal_load((const u32x4*)(ip + 256 * i + 4 * lane));
    }
    const u32 Vt = __builtin_nontemporal_load(vp + 1024 + (lane & 15));
    const u32 It = __builtin_nontemporal_load(ip + 1024 + (lane & 15));

    const u32 rot = (65536u - (blockIdx.x & 7u) * 8192u) & 0xffffu;
    u32* progw = (u32*)(smem + WPB * WAVE_B);
    if (lane == 0) progw[wib] = 0;
    char* wb = smem + wib * WAVE_B;
    u32* vr = (u32*)(wb + OFF_VR);
    char* simg = wb + OFF_S;
    u32* cnt = (u32*)(wb + OFF_CNT);
    {
        const u32x4 z = {0, 0, 0, 0};
        *(u32x4*)(simg + lane * 16) = z;
        *(u32x4*)(simg + 1024 + lane * 16) = z;
        const u32 padc = ((0xffffu - rot) & 0xffffu) * 0x10001u;
        const u32x4 f = {padc, padc, padc, padc};
        *(u32x4*)(wb + lane * 16) = f;
        *(u32x4*)(wb + 1024 + lane * 16) = f;
        if (lane < (COLS_B - 2048) / 16) *(u32x4*)(wb + 2048 + lane * 16) = f;
        cnt[lane] = 0;
#pragma unroll
        for (int i = 0; i < VR_B / 1024; ++i) *(u32x4*)(wb + OFF_VR + i * 1024 + lane * 16) = z;
        if (lane < (VR_B % 1024) / 16) *(u32x4*)(wb + OFF_VR + (VR_B / 1024) * 1024 + lane * 16) = z;
    }
    u32 rowb[16];
#pragma unroll
    for (int i = 0; i < 4; ++i)
#pragma unroll
        for (int u = 0; u < 4; ++u) {
            rowb[4 * i + u] = (((256 * i + 4 * lane + u) * 4033u) >> 14) & 0xf0u;
            asm volatile("" : "+v"(rowb[4 * i + u]));
        }
    const u32 cntb = (u32)(size_t)(lds_ptr)(char*)cnt;
    const u32 wbl = (u32)(size_t)(lds_ptr)wb;
    const u32 vzero = 0, vone = 1;
    u64 msk[17];
    u32 adr[17], rank[17];
#pragma unroll
    for (int i = 0; i < 4; ++i) {
        asm("v_cmp_neq_f16_sdwa %0, %1, %2 src0_sel:WORD_0 src1_sel:WORD_0" : "=s"(msk[4 * i + 0]) : "v"(V[i][0]), "v"(vzero));
        asm("v_cmp_neq_f16_sdwa %0, %1, %2 src0_sel:WORD_1 src1_sel:WORD_0" : "=s"(msk[4 * i + 1]) : "v"(V[i][0]), "v"(vzero));
        asm("v_cmp_neq_f16_sdwa %0, %1, %2 src0_sel:WORD_0 src1_sel:WORD_0" : "=s"(msk[4 * i + 2]) : "v"(V[i][1]), "v"(vzero));
        asm("v_cmp_neq_f16_sdwa %0, %1, %2 src0_sel:WORD_1 src1_sel:WORD_0" : "=s"(msk[4 * i + 3]) : "v"(V[i][1]), "v"(vzero));
#pragma unroll
        for (int u = 0; u < 4; ++u) adr[4 * i + u] = cntb + ((((I[i][u] + rot) >> (16 - LOGP)) & ((1u << LOGP) - 1)) << 2);
        asm volatile("s_nop 0\n\t"
                     "s_mov_b64 exec, %8\n\tds_add_rtn_u32 %0, %4, %12\n\t"
                     "s_mov_b64 exec, %9\n\tds_add_rtn_u32 %1, %5, %12\n\t"
                     "s_mov_b64 exec, %10\n\tds_add_rtn_u32 %2, %6, %12\n\t"
                     "s_mov_b64 exec, %11\n\tds_add_rtn_u32 %3, %7, %12\n\t"
                     "s_mov_b64 exec, -1"
                     : "=&v"(rank[4 * i]), "=&v"(rank[4 * i + 1]), "=&v"(rank[4 * i + 2]), "=&v"(rank[4 * i + 3])
                     : "v"(adr[4 * i]), "v"(adr[4 * i + 1]), "v"(adr[4 * i + 2]), "v"(adr[4 * i + 3]),
                       "s"(msk[4 * i]), "s"(msk[4 * i + 1]), "s"(msk[4 * i + 2]), "s"(msk[4 * i + 3]), "v"(vone)
                     : "memory");
    }
    {
        u64 m;
        asm("v_cmp_neq_f16_sdwa %0, %1, %2 src0_sel:WORD_0 src1_sel:WORD_0" : "=s"(m) : "v"(Vt), "v"(vzero));
        msk[16] = m & 0xffffull;
        adr[16] = cntb + ((((It + rot) >> (16 - LOGP)) & ((1u << LOGP) - 1)) << 2);
        asm volatile("s_nop 0\n\ts_mov_b64 exec, %2\n\tds_add_rtn_u32 %0, %1, %3\n\ts_mov_b64 exec, -1"
                     : "=&v"(rank[16]) : "v"(adr[16]), "s"(msk[16]), "v"(vone) : "memory");
    }
    const u32 c = cnt[lane];
    u32 x = c;
    x = dpp_add<0x111, 0xf>(x);
    asm volatile("s_waitcnt lgkmcnt(0)" ::: "memory");
    x = dpp_add<0x112, 0xf>(x);
    x = dpp_add<0x114, 0xf>(x);
    x = dpp_add<0x118, 0xf>(x);
    x = dpp_add<0x142, 0xa>(x);
    x = dpp_add<0x143, 0xc>(x);
    const int n = __builtin_amdgcn_readlane((int)x, 63);
    cnt[lane] = x - c;
    u32 pos[17];
#pragma unroll
    for (int j = 0; j < 17; ++j) pos[j] = *(lds_cu32)(size_t)adr[j];
    __builtin_amdgcn_sched_barrier(0);
#pragma unroll
    for (int i = 0; i < 4; ++i) {
        u32 ca[4], va[4], w[4];
#pragma unroll
        for (int u = 0; u < 4; ++u) {
            const u32 p = pos[4 * i + u] + rank[4 * i + u];
            ca[u] = wbl + 2 * p;
            va[u] = wbl + 4 * p;
            const u32 vv = V[i][u >> 1];
            w[u] = (u & 1) ? ((vv & 0xffff0000u) | rowb[4 * i + u]) : ((vv << 16) | rowb[4 * i + u]);
        }
        asm volatile("s_mov_b64 exec, %16\n\tds_write_b16 %0, %8\n\tds_write_b32 %4, %12 offset:%20\n\t"
                     "s_mov_b64 exec, %17\n\tds_write_b16 %1, %9\n\tds_write_b32 %5, %13 offset:%20\n\t"
                     "s_mov_b64 exec, %18\n\tds_write_b16 %2, %10\n\tds_write_b32 %6, %14 offset:%20\n\t"
                     "s_mov_b64 exec, %19\n\tds_write_b16 %3, %11\n\tds_write_b32 %7, %15 offset:%20\n\t"
                     "s_mov_b64 exec, -1"
                     :
                     : "v"(ca[0]), "v"(ca[1]), "v"(ca[2]), "v"(ca[3]), "v"(va[0]), "v"(va[1]), "v"(va[2]), "v"(va[3]),
                       "v"(I[i][0]), "v"(I[i][1]), "v"(I[i][2]), "v"(I[i][3]), "v"(w[0]), "v"(w[1]), "v"(w[2]), "v"(w[3]),
                       "s"(msk[4 * i]), "s"(msk[4 * i + 1]), "s"(msk[4 * i + 2]), "s"(msk[4 * i + 3]), "i"(OFF_VR)
                     : "memory");
    }
    {
        const u32 p = pos[16] + rank[16];
        const u32 ca = wbl + 2 * p, va = wbl + 4 * p, w = (Vt << 16) | (15u << 4);
        asm volatile("s_mov_b64 exec, %4\n\tds_write_b16 %0, %2\n\tds_write_b32 %1, %3 offset:%5\n\ts_mov_b64 exec, -1"
                     :: "v"(ca), "v"(va), "v"(It), "v"(w), "s"(msk[16]), "i"(OFF_VR) : "memory");
    }

    const u32 laneoff = (lane & 7) * 16;
    const u32 sconst = (lane >> 5) * 1024 + ((lane >> 3) & 3) * 256 + (2 * (lane & 3) + ((lane >> 2) & 1)) * 2;
    const char* colrd = wb + (lane >> 3) * 8;
    f32x4 acc[4];
#pragma unroll
    for (int j = 0; j < 4; ++j) acc[j] = (f32x4){0.f, 0.f, 0.f, 0.f};
    const int nsteps = (n + 31) >> 5;
#define PACE() { \
        u32 a_; \
        asm volatile("v_mov_b32 %0, %1\n\ts_nop 1\n\t" \
            "v_min_u32_dpp %0, %0, %0 row_shr:1 row_mask:0xf bank_mask:0xf\n\ts_nop 1\n\t" \
            "v_min_u32_dpp %0, %0, %0 row_shr:2 row_mask:0xf bank_mask:0xf\n\ts_nop 1\n\t" \
            "v_min_u32_dpp %0, %0, %0 row_shr:4 row_mask:0xf bank_mask:0xf\n\ts_nop 1\n\t" \
            "v_min_u32_dpp %0, %0, %0 row_shr:8 row_mask:0xf bank_mask:0xf\n\ts_nop 0" : "=&v"(a_) : "v"(pg)); \
        const int min_ = __builtin_amdgcn_readlane((int)a_, WPB - 1); \
        const int ahead_ = __builtin_amdgcn_readlane((int)pg, wib) - min_; \
        const int ahead3_ = (min_ == 0) ? 65536 : ahead_; \
        asm volatile("s_setprio 0\n\ts_cmp_lt_i32 %0, 12000\n\ts_cbranch_scc0 1f\n\t" \
            "s_setprio 1\n\ts_cmp_lt_i32 %0, 8000\n\ts_cbranch_scc0 1f\n\t" \
            "s_setprio 2\n\ts_cmp_lt_i32 %1, 4000\n\ts_cbranch_scc0 1f\n\t" \
            "s_setprio 3\n1:" :: "s"(ahead_), "s"(ahead3_) : "scc"); }
#define RDCOLS(s) (*(const u32x2*)(colrd + (s) * 64))
#define BUILD_S(bb) { const u32 sa_ = sconst + (wv & 0xffffu); \
        *(u16*)(simg + sa_) = (u16)(wv >> 16); \
        S0 = *(const u32x4*)(simg + lane * 16); S1 = *(const u32x4*)(simg + 1024 + lane * 16); \
        *(u16*)(simg + sa_) = 0; \
        wv = vr[((bb) + 1) * 64 + lane]; }
    if (nsteps > 0) {
        u32x4 gA[4], gB[4];
        u32x4 S0, S1;
        const u32x2 c0 = RDCOLS(0);
        u32x2 cB = RDCOLS(1), cA = RDCOLS(2);
        u32 wv = vr[lane];
        u32 pg = progw[lane & (WPB - 1)];
        PACE();
        issue_gather(gA, B, c0, laneoff);
        __builtin_amdgcn_sched_barrier(0);
        BUILD_S(0);
        int b = 0;
        for (; 2 * b + 2 < nsteps; ++b) {
            PACE();
            issue_gather(gB, B, cB, laneoff);
            __builtin_amdgcn_sched_barrier(0);
            compute_step(gA, S0, acc);
            __builtin_amdgcn_sched_barrier(0);
            issue_gather(gA, B, cA, laneoff);
            __builtin_amdgcn_sched_barrier(0);
            compute_step(gB, S1, acc);
            __builtin_amdgcn_sched_barrier(0);
            if (lane == 0) progw[wib] = (cA[0] + rot) & 0xffffu;
            pg = progw[lane & (WPB - 1)];
            cB = RDCOLS(2 * b + 3);
            cA = RDCOLS(2 * b + 4);
            BUILD_S(b + 1);
            __builtin_amdgcn_sched_barrier(0);
        }
        if (2 * b + 1 < nsteps) {
            issue_gather(gB, B, cB, laneoff);
            __builtin_amdgcn_sched_barrier(0);
            compute_step(gA, S0, acc);
            compute_step(gB, S1, acc);
        } else {
            compute_step(gA, S0, acc);
        }
    }
    if (lane == 0) progw[wib] = 0x7fffffffu;
    bool bad = false;
#pragma unroll
    for (int j = 0; j < 4; ++j)
#pragma unroll
        for (int r = 0; r < 4; ++r) bad |= __builtin_amdgcn_classf(acc[j][r], 0x207);
    if (__builtin_expect(__builtin_amdgcn_ballot_w64(bad) != 0, 0)) {
        rows_exact(vals, idx, (const u16*)B, (u16*)out, (size_t)tile * ROWS, ROWS, KS, lane);
    } else {
        u32x2* op = out + ((size_t)tile * ROWS + 4 * (lane >> 4)) * 16 + (lane & 7) * 2 + ((lane >> 3) & 1);
#pragma unroll
        for (int r = 0; r < 4; ++r) {
            u32x2 o;
            o[0] = cvt_pk_bf16(acc[0][r], acc[1][r]);
            o[1] = cvt_pk_bf16(acc[2][r], acc[3][r]);
            __builtin_nontemporal_store(o, op + r * 16);
        }
    }
}

__global__ __launch_bounds__(256) void spmm_ell_generic(const u16* __restrict__ vals, const u32* __restrict__ idx,
                                                        const u16* __restrict__ B, u16* __restrict__ out, int M, int K) {
    const int row = blockIdx.x * 4 + (threadIdx.x >> 6);
    if (row < M) rows_exact(vals, idx, B, out, (size_t)row, 1, K, threadIdx.x & 63);
}

extern "C" void kernel_launch(void* const* d_in, const int* in_sizes, int n_in,
                              void* d_out, int out_size, void* d_ws, size_t ws_size,
                              hipStream_t stream) {
    const u16* vals = (const u16*)d_in[0];
    const u32* idx = (const u32*)d_in[1];
    const char* B = (const char*)d_in[2];
    const int M = out_size / DD;
    const int K = in_sizes[0] / M;
    const int N = in_sizes[2] / DD;
    if (K == KS && N == 65536 && M % (ROWS * WPB) == 0) {
        spmm_ell_kernel<<<M / (ROWS * WPB), WPB * 64, 0, stream>>>(vals, idx, B, (u32x2*)d_out);
    } else {
        spmm_ell_generic<<<(M + 3) / 4, 256, 0, stream>>>(vals, idx, (const u16*)B, (u16*)d_out, M, K);
    }
}
```
